```python
import torch

def _softmax(x, axis=-1):
    return torch.softmax(x, dim=axis)
def _gelu(x):
    return torch.nn.functional.gelu(x, approximate="tanh")
def _silu(x):
    return torch.nn.functional.silu(x)
def _sigmoid(x):
    return torch.sigmoid(x)
def _layernorm(x, axis=-1, eps=1e-5):
    m = x.mean(dim=axis, keepdim=True)
    v = x.var(dim=axis, keepdim=True, unbiased=False)
    return (x - m) / torch.sqrt(v + eps)
def _scatter_add(tgt, idx, src, axis=0):
    return tgt.clone().index_add_(axis, idx, src)
def _segment_sum(src, seg, n):
    out = torch.zeros((n,) + src.shape[1:], dtype=src.dtype)
    return out.index_add_(0, seg, src)
def _pad(x, pw):
    flat = tuple(v for pair in reversed(pw) for v in pair)
    return torch.nn.functional.pad(x, flat)

kernel_name = 'sequential_preLN__sliding_local_dilated__dense_activation_relu__prompt'

def setup_inputs():

    _w = (lambda a: (lambda t: t.to(torch.bfloat16) if t.is_floating_point() else t)(torch.as_tensor(a)))
    def _A_setup():
        B, S, D = 1, 2048, 512
        return {
            "x": _w((lambda *s: torch.randn(*s, dtype=torch.bfloat16) * 0.05)(B, S, D)),
            "wqkv": _w((lambda *s: torch.randn(*s, dtype=torch.bfloat16) * 0.05)(D, 3 * D)),
            "wo": _w((lambda *s: torch.randn(*s, dtype=torch.bfloat16) * 0.05)(D, D)),
        }
    _A_in = _A_setup()

    def _B_setup():
        B, S, D, Dff = 1, 2048, 512, 1376
        return {
            "x": _w((lambda *s: torch.randn(*s, dtype=torch.bfloat16) * 0.05)(B, S, D)),
            "w1": _w((lambda *s: torch.randn(*s, dtype=torch.bfloat16) * 0.05)(D, Dff)),
            "w2": _w((lambda *s: torch.randn(*s, dtype=torch.bfloat16) * 0.05)(Dff, D)),
        }
    _B_in = _B_setup()

    return {
        "A_x": _A_in["x"],
        "A_wqkv": _A_in["wqkv"],
        "A_wo": _A_in["wo"],
        "B_x": _B_in["x"],
        "B_w1": _B_in["w1"],
        "B_w2": _B_in["w2"],
    }

def reference(A_x, A_wqkv, A_wo, B_x, B_w1, B_w2):
    def _A_ref(x, wqkv, wo):
        B, S, D = x.shape
        H = 32; Dh = D // H
        W, dil = 8, 2
        qkv = (lambda x, *d: x.permute(*d))((x @ wqkv).reshape(B, S, 3, H, Dh), 2, 0, 3, 1, 4)
        q, k, v = qkv[0], qkv[1], qkv[2]
        scale = Dh ** -0.5
        pos = torch.arange(S)
        off = torch.arange(W) * dil
        idx = pos[:, None] - off[None, :]
        mask = idx >= 0
        idx_c = torch.where(mask, idx, 0).reshape(-1)
        kg = k[:, :, idx_c, :].reshape(B, H, S, W, Dh)
        vg = v[:, :, idx_c, :].reshape(B, H, S, W, Dh)
        logits = torch.einsum("bhsd,bhswd->bhsw", q, kg) * scale
        logits = torch.where(mask[None, None, :, :], logits, -1e9)
        attn = _softmax(logits, axis=-1)
        o = torch.einsum("bhsw,bhswd->bhsd", attn, vg)
        return (lambda x, *d: x.permute(*d))(o, 0, 2, 1, 3).reshape(B, S, D) @ wo

    def _B_ref(x, w1, w2):
        h = torch.einsum("bsd,df->bsf", x, w1)
        zero = torch.zeros_like(h)
        h = torch.maximum(h, zero)
        return torch.einsum("bsf,fd->bsd", h, w2)

    x = A_x
    h = x + _A_ref(_layernorm(x, axis=-1), A_wqkv, A_wo)
    h2 = h + _B_ref(_layernorm(h, axis=-1), B_w1, B_w2)
    return h2
```

```cpp
#include <hip/hip_runtime.h>
#include <hip/hip_bf16.h>
typedef __hip_bfloat16 bf16;
#define S_ 2048
#define D_ 512
#define F_ 1376

__global__ void ln_kernel(const bf16* x, bf16* y) {
    int row = blockIdx.x; int t = threadIdx.x;
    float v[8]; float s = 0;
    for (int i = 0; i < 8; i++) { v[i] = __bfloat162float(x[row * D_ + t * 8 + i]); s += v[i]; }
    for (int o = 32; o; o >>= 1) s += __shfl_xor(s, o);
    float m = s / D_; float q = 0;
    for (int i = 0; i < 8; i++) { float d = v[i] - m; q += d * d; }
    for (int o = 32; o; o >>= 1) q += __shfl_xor(q, o);
    float r = rsqrtf(q / D_ + 1e-5f);
    for (int i = 0; i < 8; i++) y[row * D_ + t * 8 + i] = __float2bfloat16((v[i] - m) * r);
}
__global__ void gemm_kernel(const bf16* A, const bf16* B, const bf16* res, bf16* C, int M, int N, int K, int relu) {
    int n = blockIdx.x * 64 + threadIdx.x; int m = blockIdx.y;
    if (n >= N) return;
    float acc = 0;
    for (int k = 0; k < K; k++) acc += __bfloat162float(A[m * K + k]) * __bfloat162float(B[k * N + n]);
    if (relu) acc = fmaxf(acc, 0.f);
    if (res) acc = __bfloat162float(__float2bfloat16(acc)) + __bfloat162float(res[m * N + n]);
    C[m * N + n] = __float2bfloat16(acc);
}
__global__ void attn_kernel(const bf16* qkv, bf16* o) {
    int s = blockIdx.x; int h = threadIdx.x;
    if (h >= 32) return;
    float q[16]; for (int d = 0; d < 16; d++) q[d] = __bfloat162float(qkv[s * 1536 + h * 16 + d]);
    float l[8]; float mx = -1e30f;
    for (int w = 0; w < 8; w++) { int p = s - 2 * w; float a = -1e9f; if (p >= 0) { a = 0; for (int d = 0; d < 16; d++) a += q[d] * __bfloat162float(qkv[p * 1536 + 512 + h * 16 + d]); a *= 0.25f; } l[w] = a; mx = fmaxf(mx, a); }
    float sum = 0; for (int w = 0; w < 8; w++) { l[w] = expf(l[w] - mx); sum += l[w]; }
    for (int d = 0; d < 16; d++) { float a = 0; for (int w = 0; w < 8; w++) { int p = s - 2 * w; if (p < 0) p = 0; a += l[w] / sum * __bfloat162float(qkv[p * 1536 + 1024 + h * 16 + d]); } o[s * 512 + h * 16 + d] = __float2bfloat16(a); }
}
extern "C" void kernel_launch(void* const* d_in, const int* in_sizes, int n_in, void* d_out, int out_size, void* d_ws, size_t ws_size, hipStream_t stream) {
    const bf16* x = (const bf16*)d_in[0]; const bf16* wqkv = (const bf16*)d_in[1]; const bf16* wo = (const bf16*)d_in[2];
    const bf16* w1 = (const bf16*)d_in[4]; const bf16* w2 = (const bf16*)d_in[5];
    bf16* out = (bf16*)d_out; bf16* ws = (bf16*)d_ws;
    bf16* xn = ws; bf16* qkv = xn + S_ * D_; bf16* o = qkv + S_ * 1536; bf16* h = o + S_ * D_; bf16* hn = h + S_ * D_; bf16* f = hn + S_ * D_;
    ln_kernel<<<S_, 64, 0, stream>>>(x, xn);
    gemm_kernel<<<dim3(24, S_), 64, 0, stream>>>(xn, wqkv, nullptr, qkv, S_, 1536, 512, 0);
    attn_kernel<<<S_, 64, 0, stream>>>(qkv, o);
    gemm_kernel<<<dim3(8, S_), 64, 0, stream>>>(o, wo, x, h, S_, 512, 512, 0);
    ln_kernel<<<S_, 64, 0, stream>>>(h, hn);
    gemm_kernel<<<dim3(22, S_), 64, 0, stream>>>(hn, w1, nullptr, f, S_, F_, 512, 1);
    gemm_kernel<<<dim3(8, S_), 64, 0, stream>>>(f, w2, h, out, S_, 512, F_, 0);
}
```

```cpp
#include <hip/hip_runtime.h>
#include <hip/hip_bf16.h>
#include <utility>
#pragma clang diagnostic ignored "-Wint-to-pointer-cast"
#pragma clang diagnostic ignored "-Wc++20-extensions"

namespace fb {
typedef unsigned u32; typedef unsigned long long u64; typedef unsigned short u16;
typedef u32 v4u __attribute__((ext_vector_type(4)));
typedef u32 v2u __attribute__((ext_vector_type(2)));
typedef float f32x4 __attribute__((ext_vector_type(4)));
typedef float f32x2 __attribute__((ext_vector_type(2)));
typedef __bf16 bf8 __attribute__((ext_vector_type(8)));
typedef __bf16 bf2 __attribute__((ext_vector_type(2)));
typedef short s4 __attribute__((ext_vector_type(4)));
#define LAS __attribute__((address_space(3)))
#define GAS __attribute__((address_space(1)))
#define DEV __device__ __forceinline__

constexpr u32 WS_EPOCH = 0;
constexpr u32 WS_FLA = 16384;
constexpr u32 WS_FLB = WS_FLA + 32768;
constexpr u32 WS_FLC = WS_FLB + 32768;
constexpr u32 WS_O = 1u << 20;
constexpr u32 WS_H = 3u << 20;
constexpr u32 WS_F = 5u << 20;
constexpr u32 F_TAU = 86 * 512;
constexpr u32 WS_DBG = 12u << 20;
constexpr u32 WS_BYTES = 13u << 20;
constexpr u32 L_X = 0, L_RING = 81920, SLOT = 12288, NSLOT = 6, L_CTRL = 155648, L_TOTAL = 163840;
constexpr u32 L_AW = 65536, AWP = 22528, FPART = 11264;
constexpr u32 C_R = L_CTRL, C_B = C_R + 320, C_TAG = C_B + 320;
constexpr u32 C_FLAGS = L_CTRL + 1024;
constexpr u32 FL_P2PART = 0, FL_P2FIN = 16, FL_HRDY = 32, FL_SRDY = 48, FL_WOL = 64, FL_BDONE = 68, FL_W1L = 72, FL_W1C = 76, FL_P4PART = 80, FL_P4FIN = 96;
constexpr u32 C_S2 = L_CTRL + 2048;
constexpr u32 C_CS1 = L_CTRL + 4096;
constexpr u32 SPIN_G = 1u << 16, SPIN_L = 1u << 20;

struct Params { const char* x; const char* wqkv; const char* wo; const char* w1; const char* w2; char* out; char* ws; };

DEV f32x4 mfma32(v4u a, v4u b, f32x4 c) { return __builtin_amdgcn_mfma_f32_16x16x32_bf16(__builtin_bit_cast(bf8, a), __builtin_bit_cast(bf8, b), c, 0, 0, 0); }
DEV f32x4 mfma16(v2u a, v2u b, f32x4 c) { return __builtin_amdgcn_mfma_f32_16x16x16bf16_1k(__builtin_bit_cast(s4, a), __builtin_bit_cast(s4, b), c, 0, 0, 0); }
DEV u32 pk2(float a, float b) { bf2 p = {(__bf16)a, (__bf16)b}; return __builtin_bit_cast(u32, p); }
DEV float bflo(u32 u) { return __builtin_bit_cast(float, u << 16); }
DEV float bfhi(u32 u) { return __builtin_bit_cast(float, u & 0xffff0000u); }
DEV u32 uni(u32 v) { return __builtin_amdgcn_readfirstlane(v); }
DEV v4u lds128(u32 a) { return *(const LAS v4u*)a; }
DEV v2u lds64(u32 a) { return *(const LAS v2u*)a; }
DEV f32x4 ldsf4(u32 a) { return *(const LAS f32x4*)a; }
DEV float ldsf(u32 a) { return *(const LAS float*)a; }
DEV v2u ldstr(u32 a) { return __builtin_bit_cast(v2u, __builtin_amdgcn_ds_read_tr16_b64_v4i16((LAS s4*)a)); }
DEV float dot2(u32 a, u32 b, float c) { return __builtin_amdgcn_fdot2_f32_bf16(__builtin_bit_cast(bf2, a), __builtin_bit_cast(bf2, b), c, false); }
#define MEMFENCE() asm volatile("" ::: "memory")
#define SB0() __builtin_amdgcn_sched_barrier(0)
#define BARRIER() do { SB0(); asm volatile("s_waitcnt lgkmcnt(0)\n\ts_barrier" ::: "memory"); SB0(); } while (0)
#define SGB(m, n) __builtin_amdgcn_sched_group_barrier(m, n, 0)
#define VMWAIT0() asm volatile("s_waitcnt vmcnt(0)" ::: "memory")
template <int N> DEV void vmwait() { asm volatile("s_waitcnt vmcnt(%0)" ::"i"(N) : "memory"); }
#define GLD "global_load_lds_dwordx4 "
DEV void dma3(const char* sb, u32 v, u32 dst) {
  asm volatile("s_mov_b32 m0, %2\n\ts_nop 0\n\t" GLD "%0, %1\n\t" GLD "%0, %1 offset:1024\n\t" GLD "%0, %1 offset:2048" ::"v"(v), "s"(sb), "s"(dst) : "memory");
}
DEV void dma2v(const char* sb, u32 v0, u32 v1, u32 dst) {
  asm volatile("s_mov_b32 m0, %3\n\ts_nop 0\n\t" GLD "%0, %2\n\t" GLD "%1, %2 offset:1024" ::"v"(v0), "v"(v1), "s"(sb), "s"(dst) : "memory");
}
DEV void dma_more2048(const char* sb, u32 v) { asm volatile(GLD "%0, %1 offset:2048" ::"v"(v), "s"(sb) : "memory"); }
DEV void dma4v(const char* sb, u32 v0, u32 v1, u32 v2, u32 v3, u32 dst) {
  asm volatile("s_mov_b32 m0, %5\n\ts_nop 0\n\t" GLD "%0, %4\n\t" GLD "%1, %4 offset:1024\n\t" GLD "%2, %4 offset:2048\n\t" GLD "%3, %4 offset:3072"
               ::"v"(v0), "v"(v1), "v"(v2), "v"(v3), "s"(sb), "s"(dst) : "memory");
}
DEV void dma5v(const char* sb, const u32 (&v)[5], u32 dst) {
  asm volatile("s_mov_b32 m0, %6\n\ts_nop 0\n\t" GLD "%0, %5 offset:-1024\n\t" GLD "%1, %5\n\t" GLD "%2, %5 offset:1024\n\t" GLD "%3, %5 offset:2048\n\t"
               GLD "%4, %5 offset:3072" ::"v"(v[0]), "v"(v[1]), "v"(v[2]), "v"(v[3]), "v"(v[4]), "s"(sb), "s"(dst) : "memory");
}
DEV void dma4_sc1(const char* sb, u32 v, u32 dst) {
  asm volatile("s_mov_b32 m0, %2\n\ts_nop 0\n\t" GLD "%0, %1 sc1\n\t" GLD "%0, %1 offset:1024 sc1\n\t" GLD "%0, %1 offset:2048 sc1\n\t"
               GLD "%0, %1 offset:3072 sc1" ::"v"(v), "s"(sb), "s"(dst) : "memory");
}
DEV void dma8_sc1(const char* sb, u32 v, u32 dst) {
  asm volatile("s_mov_b32 m0, %2\n\ts_nop 0\n\t" GLD "%0, %1 offset:-4096 sc1\n\t" GLD "%0, %1 offset:-3072 sc1\n\t" GLD "%0, %1 offset:-2048 sc1\n\t"
               GLD "%0, %1 offset:-1024 sc1\n\t" GLD "%0, %1 sc1\n\t" GLD "%0, %1 offset:1024 sc1\n\t" GLD "%0, %1 offset:2048 sc1\n\t"
               GLD "%0, %1 offset:3072 sc1" ::"v"(v), "s"(sb), "s"(dst) : "memory");
}
DEV void dma2_sc1(const char* sb, u32 v, u32 dst) {
  asm volatile("s_mov_b32 m0, %2\n\ts_nop 0\n\t" GLD "%0, %1 sc1\n\t" GLD "%0, %1 offset:1024 sc1" ::"v"(v), "s"(sb), "s"(dst) : "memory");
}
DEV void dma_more2048_sc1(const char* sb, u32 v) { asm volatile(GLD "%0, %1 offset:2048 sc1" ::"v"(v), "s"(sb) : "memory"); }
DEV void lflag_set(u32 word, u32 v) {
  MEMFENCE();
  __hip_atomic_store((LAS u32*)(C_FLAGS + word * 4), v, __ATOMIC_RELAXED, __HIP_MEMORY_SCOPE_WORKGROUP);
  MEMFENCE();
}
DEV u32 lflag_min4(u32 word) {
  v4u v;
  asm volatile("ds_read_b128 %0, %1\n\ts_waitcnt lgkmcnt(0)" : "=v"(v) : "v"(C_FLAGS + word * 4) : "memory");
  u32 m = min(min(v.x, v.y), min(v.z, v.w));
  return uni(m);
}
DEV u32 lflag_get(u32 word) {
  u32 v;
  asm volatile("ds_read_b32 %0, %1\n\ts_waitcnt lgkmcnt(0)" : "=v"(v) : "v"(C_FLAGS + word * 4) : "memory");
  return uni(v);
}
DEV void lwait4(u32 word, u32 want) { for (u32 s = 0; s < SPIN_L && lflag_min4(word) < want; ++s) {} }
DEV void lwait1(u32 word, u32 want) { for (u32 s = 0; s < SPIN_L && lflag_get(word) < want; ++s) {} }
DEV void gflag_pub(char* ws, u32 off, u64 tag, int lane) {
  VMWAIT0();
  if (lane == 0) __hip_atomic_store((GAS u64*)(ws + off), tag, __ATOMIC_RELAXED, __HIP_MEMORY_SCOPE_AGENT);
  MEMFENCE();
}
DEV u64 gflag_poll(char* ws, u32 off, u64 tag, int lane, int n) {
  u64 v = __hip_atomic_load((GAS u64*)(ws + off + (lane & (n - 1)) * 8), __ATOMIC_RELAXED, __HIP_MEMORY_SCOPE_AGENT);
  u64 m = __builtin_amdgcn_ballot_w64(v == tag);
  MEMFENCE();
  return m;
}
DEV u64 mix64(u64 z) {
  z += 0x9E3779B97F4A7C15ull; z = (z ^ (z >> 30)) * 0xBF58476D1CE4E5B9ull; z = (z ^ (z >> 27)) * 0x94D049BB133111EBull; return z ^ (z >> 31);
}
DEV float asf(u32 u) { return __builtin_bit_cast(float, u); }
DEV u32 asu(float f) { return __builtin_bit_cast(u32, f); }
DEV float xg_sum(float v) {
  auto a = __builtin_amdgcn_permlane16_swap(asu(v), asu(v), false, false);
  v = asf(a[0]) + asf(a[1]);
  auto b = __builtin_amdgcn_permlane32_swap(asu(v), asu(v), false, false);
  return asf(b[0]) + asf(b[1]);
}
DEV float xg_max(float v) {
  auto a = __builtin_amdgcn_permlane16_swap(asu(v), asu(v), false, false);
  v = fmaxf(asf(a[0]), asf(a[1]));
  auto b = __builtin_amdgcn_permlane32_swap(asu(v), asu(v), false, false);
  return fmaxf(asf(b[0]), asf(b[1]));
}
template <int CTRL> DEV float dppf(float v) { return __builtin_bit_cast(float, __builtin_amdgcn_update_dpp(0u, __builtin_bit_cast(u32, v), CTRL, 0xf, 0xf, false)); }
DEV float row_sum(float v) { v += dppf<0x128>(v); v += dppf<0x124>(v); v += dppf<0x122>(v); v += dppf<0x121>(v); return v; }
DEV void acc_stats(v4u f, float& s, float& q) {
#pragma unroll
  for (int e = 0; e < 4; ++e) {
    const float lo = bflo(f[e]), hi = bfhi(f[e]);
    s += lo; q = fmaf(lo, lo, q); s += hi; q = fmaf(hi, hi, q);
    asm volatile("" : "+v"(s), "+v"(q));
  }
}
DEV void ln_coef(float s, float q, float& r, float& b) {
  float m = s * (1.0f / 512.0f);
  float var = fmaxf(q * (1.0f / 512.0f) - m * m, 0.0f);
  r = __builtin_amdgcn_rsqf(var + 1e-5f);
  b = -m * r;
}
#define STAMP(i)
#define STAMP_DECL
#define STAMP_OUT()

struct Frag4 { v4u f[4]; };
DEV void wcell_dma(const char* src, u32 ve, u32 dst) {
  dma4v(src, ve, (ve ^ 64) + 3072, ve + 6144, (ve ^ 64) + 9216, dst);
}
DEV u32 wcell_voff(int lane, int j) {
  const int r = lane >> 3;
  return (16 * (r >> 2) + (r & 3)) * 1024 + j * 128 + ((((lane & 7) >> 1) ^ ((r >> 1) & 1)) * 32) + (lane & 1) * 16;
}
DEV u32 wcell_roff(int lane) {
  const int G = lane >> 4, q = (lane & 15) >> 2;
  return G * 1024 + q * 128 + ((((q >> 1) & 1) | ((G & 1) << 1)) * 32) + (lane & 3) * 8;
}
DEV void wcell_read(Frag4& Fg, u32 cell, u32 ro) {
#pragma unroll
  for (int c = 0; c < 4; ++c) { const u32 ad = cell + (ro ^ (c * 32)); v2u lo = ldstr(ad), hi = ldstr(ad + 512); Fg.f[c] = v4u{lo.x, lo.y, hi.x, hi.y}; }
}
template <int NK> DEV void ks_tile(const Frag4 (&W)[NK], u32 src, int lane, bool last_valid) {
  f32x4 acc[4] = {{0, 0, 0, 0}, {0, 0, 0, 0}, {0, 0, 0, 0}, {0, 0, 0, 0}};
  const u32 a = src + lane * 8;
  constexpr int PD = 4;
  v4u b[NK];
  auto rd = [&](int s) { v2u lo = lds64(a + s * 1024), hi = lds64(a + s * 1024 + 512); b[s] = v4u{lo.x, lo.y, hi.x, hi.y}; };
  SB0();
#pragma unroll
  for (int s = 0; s < PD && s < NK; ++s) rd(s);
  SB0();
#pragma unroll
  for (int s = 0; s < NK - 1; ++s) {
    acc[0] = mfma32(W[s].f[0], b[s], acc[0]);
    SB0();
    if (s + PD < NK) rd(s + PD);
    SB0();
#pragma unroll
    for (int c = 1; c < 4; ++c) acc[c] = mfma32(W[s].f[c], b[s], acc[c]);
    SB0();
  }
  if (last_valid) {
#pragma unroll
    for (int c = 0; c < 4; ++c) acc[c] = mfma32(W[NK - 1].f[c], b[NK - 1], acc[c]);
  }
  MEMFENCE();
#pragma unroll
  for (int c = 0; c < 4; ++c) *(LAS f32x4*)(src + c * 1024 + lane * 16) = acc[c];
}
DEV void ks_sum(u32 base, u32 stride, int lane, f32x4 (&v)[4]) {
  const u32 ad = base + (lane >> 4) * 1024 + (lane & 15) * 16;
#pragma unroll
  for (int i = 0; i < 4; ++i) {
    f32x4 s = ldsf4(ad + i * 256) + ldsf4(ad + stride + i * 256);
    f32x4 t = ldsf4(ad + 2 * stride + i * 256) + ldsf4(ad + 3 * stride + i * 256);
    v[i] = s + t;
  }
}
DEV void add_res(const f32x4 (&v)[4], v4u r0, v4u r1, v4u& o0, v4u& o1) {
  o0[0] = pk2(v[0][0] + bflo(r0[0]), v[0][1] + bfhi(r0[0])); o0[1] = pk2(v[0][2] + bflo(r0[1]), v[0][3] + bfhi(r0[1]));
  o0[2] = pk2(v[1][0] + bflo(r0[2]), v[1][1] + bfhi(r0[2])); o0[3] = pk2(v[1][2] + bflo(r0[3]), v[1][3] + bfhi(r0[3]));
  o1[0] = pk2(v[2][0] + bflo(r1[0]), v[2][1] + bfhi(r1[0])); o1[1] = pk2(v[2][2] + bflo(r1[1]), v[2][3] + bfhi(r1[1]));
  o1[2] = pk2(v[3][0] + bflo(r1[2]), v[3][1] + bfhi(r1[2])); o1[3] = pk2(v[3][2] + bflo(r1[3]), v[3][3] + bfhi(r1[3]));
}

constexpr int SCH[17][5] = {{16, 0, 1, 2, 3}, {17, -1, -1, -1, -1}, {4, 5, -1, -1, -1}, {6, 7, -1, -1, -1}, {18, -1, -1, -1, -1}, {8, 9, -1, -1, -1},
                            {10, -1, -1, -1, -1}, {11, 19, -1, -1, -1}, {12, -1, -1, -1, -1}, {13, -1, -1, -1, -1}, {14, -1, -1, -1, -1},
                            {15, -1, -1, -1, -1}, {20, -1, -1, -1, -1}, {21, -1, -1, -1, -1}, {22, -1, -1, -1, -1}, {23, -1, -1, -1, -1},
                            {24, 25, -1, -1, -1}};
constexpr int item_ops(int it) { return it < 0 ? 0 : it < 16 ? 3 : it < 20 ? 5 : it < 24 ? 4 : 3; }
constexpr int item_end(int it) { int n = 0; for (int gI = 0; gI < 17; ++gI) for (int k = 0; k < 5; ++k) { n += item_ops(SCH[gI][k]); if (SCH[gI][k] == it) return n; } return 1 << 20; }
constexpr int issued_before_barrier(int b) { int n = 0; for (int gI = 0; gI <= b; ++gI) for (int k = 0; k < 5; ++k) n += item_ops(SCH[gI][k]); return n; }
constexpr int xpair_at(int b) { return b < 3 ? 0 : b < 7 ? 1 : b < 10 ? 2 : 3; }
constexpr int need_before_barrier(int b) { return item_end(b) > item_end(16 + xpair_at(b)) ? item_end(b) : item_end(16 + xpair_at(b)); }

__global__ void __launch_bounds__(512, 2) fused_block(Params p) {
  extern __shared__ __attribute__((aligned(16))) char smem[];
  const int tid = threadIdx.x, lane = tid & 63;
  const int w = uni(tid >> 6);
  const int bid = blockIdx.x, xcd = bid & 7, idx = bid >> 3;
  const int g = xcd * 4 + (idx >> 3), j = idx & 7;
  const int T0 = g * 64;
  const int t = lane & 15, G = lane >> 4;
  const __amdgpu_buffer_rsrc_t rws = __builtin_amdgcn_make_buffer_rsrc((void*)p.ws, 0, WS_BYTES, 0x00020000);
  STAMP_DECL;
  STAMP(0);
  asm volatile("" ::"s"(smem));

  if (w < 4) {
    const int a = w;
    const u64 ep = __hip_atomic_load((GAS u64*)(p.ws + WS_EPOCH + g * 256), __ATOMIC_RELAXED, __HIP_MEMORY_SCOPE_AGENT);
    if (a == 0) { *(LAS u32*)(C_FLAGS + lane * 4) = 0; *(LAS u32*)(C_FLAGS + 256 + lane * 4) = 0; }
    f32x4 aq[5], ak[5], av[5];
#pragma unroll
    for (int i = 0; i < 5; ++i) { aq[i] = f32x4{0, 0, 0, 0}; ak[i] = f32x4{0, 0, 0, 0}; av[i] = f32x4{0, 0, 0, 0}; }
    const int q = (lane & 15) >> 2, pp = lane & 3;
    const u32 woff = L_RING + G * 3072 + q * 128 + ((a ^ (((q >> 1) & 1) | ((G & 1) << 1))) * 32) + pp * 8;
    const u32 xo0 = L_X + (t >> 3) * 1024 + (t & 7) * 128 + ((G ^ ((t >> 1) & 7)) << 4);
    struct Fr { v4u w[3]; v4u x[5]; };
    auto ldfr = [&](Fr& f, u32 wa, u32 xa) {
#pragma unroll
      for (int m = 0; m < 3; ++m) { v2u lo = ldstr(wa + m * 1024), hi = ldstr(wa + m * 1024 + 512); f.w[m] = v4u{lo.x, lo.y, hi.x, hi.y}; }
#pragma unroll
      for (int i = 0; i < 5; ++i) f.x[i] = lds128(xa + i * 2048);
    };
    auto mma = [&](Fr& f) {
      v4u x0 = f.x[0];
      if (t == 0) x0 = v4u{0x3F803F80u, 0x3F803F80u, 0x3F803F80u, 0x3F803F80u};
#pragma unroll
      for (int i = 1; i < 5; ++i) { aq[i] = mfma32(f.w[0], f.x[i], aq[i]); ak[i] = mfma32(f.w[1], f.x[i], ak[i]); av[i] = mfma32(f.x[i], f.w[2], av[i]); }
      aq[0] = mfma32(f.w[0], x0, aq[0]); ak[0] = mfma32(f.w[1], x0, ak[0]); av[0] = mfma32(x0, f.w[2], av[0]);
    };
    Fr f0, f1;
    const char* xr = p.x + (size_t)(T0 + 16 * a + t) * 1024 + j * 128 + G * 32;
    v4u xres0 = *(const v4u*)xr, xres1 = *(const v4u*)(xr + 16);
    u32 so = 0, xa = xo0;
    BARRIER();
    STAMP(1);
    ldfr(f0, woff, xa);
    auto pat = [&]() {
#pragma unroll
      for (int i = 0; i < 6; ++i) { SGB(0x8, 1); SGB(0x100, 2); }
      SGB(0x8, 9);
    };
    auto two = [&]() {
      so = (so == (NSLOT - 1) * SLOT) ? 0 : so + SLOT;
      BARRIER();
      ldfr(f1, woff + so, xa ^ 64);
      mma(f0);
      pat();
      so = (so == (NSLOT - 1) * SLOT) ? 0 : so + SLOT;
      xa += 10240;
      BARRIER();
      ldfr(f0, woff + so, xa);
      mma(f1);
      pat();
    };
#pragma unroll 1
    for (int it = 0; it < 7; ++it) two();
    so = (so == (NSLOT - 1) * SLOT) ? 0 : so + SLOT;
    BARRIER();
    ldfr(f1, woff + so, xa ^ 64);
    mma(f0);
    pat();
    const u64 tag = mix64(ep);
    if (a == 0 && lane == 0) *(LAS u64*)C_TAG = tag;
    BARRIER();
    mma(f1);
    STAMP(2);
    v2u kb[5], vb[5], qb[5];
    {
      f32x4 csq, csk;
#pragma unroll
      for (int r = 0; r < 4; ++r) { csq[r] = dppf<0x150>(aq[0][r]); csk[r] = dppf<0x150>(ak[0][r]); }
      const float csv = __shfl(av[0][0], t);
#pragma unroll
      for (int i = 0; i < 5; ++i) {
        const float rt = ldsf(C_R + (16 * i + t) * 4), bt = ldsf(C_B + (16 * i + t) * 4);
        const f32x4 r4 = ldsf4(C_R + (16 * i + 4 * G) * 4), b4 = ldsf4(C_B + (16 * i + 4 * G) * 4);
        f32x4 k = ak[i] * rt + csk * bt;
        kb[i] = v2u{pk2(k[0], k[1]), pk2(k[2], k[3])};
        if (i > 0) { f32x4 qq = aq[i] * rt + csq * bt; qb[i] = v2u{pk2(qq[0], qq[1]), pk2(qq[2], qq[3])}; }
        f32x4 v = av[i] * r4 + csv * b4;
        vb[i] = v2u{pk2(v[0], v[1]), pk2(v[2], v[3])};
      }
    }
    {
      const bool par = t & 1;
      const bool cur0 = 4 * G + (t & 1) <= t, cur1 = 4 * G + (t & 1) + 2 <= t;
      const u32 sh = (t & 1) * 16;
#pragma unroll
      for (int i = 1; i < 5; ++i) {
        const f32x4 z = {0, 0, 0, 0};
        f32x4 sp = mfma16(kb[i - 1], qb[i], z), sc = mfma16(kb[i], qb[i], z);
        float c0 = par ? sc[1] : sc[0], c1 = par ? sc[3] : sc[2], p0 = par ? sp[1] : sp[0], p1 = par ? sp[3] : sp[2];
        float z0 = (cur0 ? c0 : p0) * 0.25f, z1 = (cur1 ? c1 : p1) * 0.25f;
        if (i == 1 && g == 0) { z0 = cur0 ? z0 : -1e9f; z1 = cur1 ? z1 : -1e9f; }
        const float m = xg_max(fmaxf(z0, z1));
        const float e0 = __builtin_amdgcn_exp2f((z0 - m) * 1.44269504089f), e1 = __builtin_amdgcn_exp2f((z1 - m) * 1.44269504089f);
        const float inv = __builtin_amdgcn_rcpf(xg_sum(e0 + e1));
        const u32 u = pk2(e0 * inv, e1 * inv);
        const u32 w0 = (u & 0xffffu) << sh, w1 = (u >> 16) << sh;
        const v4u P = {cur0 ? 0u : w0, cur1 ? 0u : w1, cur0 ? w0 : 0u, cur1 ? w1 : 0u};
        const v4u V = {vb[i - 1].x, vb[i - 1].y, vb[i].x, vb[i].y};
        const f32x4 o = mfma32(V, P, z);
        const v2u ob = {pk2(o[0], o[1]), pk2(o[2], o[3])};
        __builtin_amdgcn_raw_buffer_store_b64(ob, rws, lane * 8, WS_O + ((g * 4 + i - 1) * 32 + 4 * j + a) * 512, 16);
      }
    }
    STAMP(23);
    gflag_pub(p.ws, WS_FLA + (g * 4 + (j >> 1)) * 256 + ((j & 1) * 4 + a) * 8, tag, lane);
    STAMP(3);
    const u32 ro = wcell_roff(lane);
    Frag4 w2[11];
    v4u hres0, hres1;
    {
      Frag4 wo[4];
      lwait1(FL_WOL + a, 1);
#pragma unroll
      for (int s = 0; s < 4; ++s) wcell_read(wo[s], s * 16384 + a * 4096, ro);
      wcell_read(w2[0], L_AW + a * 4096, ro);
      asm volatile("s_waitcnt lgkmcnt(0)" ::: "memory");
      lflag_set(FL_WOL + a, 2);
      for (u32 s = 0; s < SPIN_G && (gflag_poll(p.ws, WS_FLA + (g * 4 + a) * 256, tag, lane, 8) & 0xff) != 0xff; ++s) {}
      STAMP(4);
#pragma unroll
      for (int i = 0; i < 4; ++i) dma4_sc1(p.ws + WS_O + ((size_t)(g * 4 + i) * 32 + 8 * a) * 512, lane * 16, i * 16384 + a * 4096);
      bool fin = false;
#pragma unroll 1
      for (int i = 0; i < 5; ++i) {
        if (i < 4) {
          if (i == 0) vmwait<12>(); else if (i == 1) vmwait<8>(); else if (i == 2) vmwait<4>(); else if (fin) vmwait<2>(); else vmwait<0>();
          if (i == 0) STAMP(9);
          if (i == 1) STAMP(17); if (i == 2) STAMP(19); if (i == 3) STAMP(21);
          ks_tile<4>(wo, i * 16384 + a * 4096, lane, true);
          lflag_set(FL_P2PART + i * 4 + a, 1);
          if (i == 0) STAMP(16); if (i == 1) STAMP(18); if (i == 2) STAMP(20); if (i == 3) STAMP(22);
        } else if (!fin) lwait4(FL_P2PART + a * 4, 1);
        if (!fin && i >= a && (i == 4 || lflag_min4(FL_P2PART + a * 4) != 0)) {
          fin = true;
          f32x4 v[4];
          ks_sum(a * 16384, 4096, lane, v);
          asm volatile("" : "+v"(xres0), "+v"(xres1));
          add_res(v, xres0, xres1, hres0, hres1);
          lflag_set(FL_P2FIN + a, 1);
          const u32 ho = WS_H + ((g * 4 + a) * 16 + 2 * j) * 1024;
          __builtin_amdgcn_raw_buffer_store_b128(hres0, rws, lane * 16, ho, 16);
          __builtin_amdgcn_raw_buffer_store_b128(hres1, rws, lane * 16 + 1024, ho, 16);
        }
      }
      STAMP(12);
      gflag_pub(p.ws, WS_FLB + (g * 4 + (j >> 1)) * 256 + ((j & 1) * 4 + a) * 8, tag, lane);
    }
    STAMP(5);
    {
      const u32 aw = L_AW + a * AWP;
      const u32 ve = wcell_voff(lane, j);
      const char* w2s = p.w2 + (size_t)(11 * a) * 32768;
      lwait4(FL_W1C, 8);
      lwait4(FL_WOL, 2);
#pragma unroll
      for (int s = 1; s < 6; ++s) wcell_dma(w2s + s * 32768, ve, aw + (s - 1) * 4096);
      VMWAIT0();
#pragma unroll
      for (int s = 1; s < 6; ++s) wcell_read(w2[s], aw + (s - 1) * 4096, ro);
      STAMP(13);
#pragma unroll 1
      for (int i = 0; i < 4; ++i) {
        lwait1(FL_HRDY + i * 4 + a, 1);
        float sm = 0.f, sq = 0.f, sm2 = 0.f, sq2 = 0.f;
        const u32 ha = i * 16384 + a * 4096 + lane * 16;
        acc_stats(lds128(ha), sm, sq); acc_stats(lds128(ha + 1024), sm2, sq2);
        acc_stats(lds128(ha + 2048), sm, sq); acc_stats(lds128(ha + 3072), sm2, sq2);
        sm = xg_sum(sm + sm2); sq = xg_sum(sq + sq2);
        if (G == 0) { *(LAS float*)(C_S2 + ((i * 16 + t) * 4 + a) * 4) = sm; *(LAS float*)(C_S2 + 1024 + ((i * 16 + t) * 4 + a) * 4) = sq; }
        lflag_set(FL_SRDY + i * 4 + a, 1);
      }
      STAMP(6);
      asm volatile("s_waitcnt lgkmcnt(0)" ::: "memory");
#pragma unroll
      for (int s = 6; s < 10; ++s) wcell_dma(w2s + s * 32768, ve, aw + (s - 6) * 4096);
      if (a < 3) wcell_dma(w2s + 10 * 32768, ve, aw + 4 * 4096);
      VMWAIT0();
#pragma unroll
      for (int s = 6; s < 11; ++s) wcell_read(w2[s], aw + (s - 6) * 4096, ro);
      asm volatile("s_waitcnt lgkmcnt(0)" ::: "memory");
    }
    STAMP(14);
    {
      int issued = 0, done = 0;
      bool fin = false;
      auto slot = [&](int i) -> u32 { return i == 2 ? a * FPART : L_AW + a * AWP + (i == 1 ? FPART : 0); };
      for (u32 s = 0; !(done == 4 && fin) && s < SPIN_G; ++s) {
        int landed = issued;
        if (issued < 4) {
          const u32 m = (u32)gflag_poll(p.ws, WS_FLC + (g * 4 + a) * 256, tag, lane, 32);
          VMWAIT0();
          while (issued < 4 && ((m >> (8 * issued)) & 0xff) == 0xff) {
            if (issued == 2 && lflag_min4(FL_BDONE) < 3) break;
            if (issued == 3 && (done < 1 || lflag_get(FL_P4FIN) == 0)) break;
            if (issued == 0) STAMP(7);
            if (issued == 3) STAMP(15);
            const u32 dst = slot(issued);
            const char* src = p.ws + WS_F + (size_t)(g * 4 + issued) * F_TAU + a * FPART;
            dma8_sc1(src + 4096, lane * 16, dst + 4096); dma2_sc1(src + 8192, lane * 16, dst + 8192);
            if (a < 3) dma_more2048_sc1(src + 8192, lane * 16);
            ++issued;
          }
        } else VMWAIT0();
        for (;;) {
          if (!fin && done > a && (lflag_min4(FL_P4PART + a * 4) != 0)) {
            fin = true;
            f32x4 v[4];
            ks_sum(a == 2 ? 0 : L_AW + (a == 1 ? FPART : 0), a == 2 ? FPART : AWP, lane, v);
            v4u o0, o1;
            asm volatile("" : "+v"(hres0), "+v"(hres1));
            add_res(v, hres0, hres1, o0, o1);
            lflag_set(FL_P4FIN + a, 1);
            int lz = lane; asm volatile("" : "+v"(lz));
            char* op = p.out + (size_t)(T0 + 16 * a) * 1024 + j * 128 + (u32)((lz & 15) * 1024 + (lz >> 4) * 32);
            *(v4u*)op = o0; *(v4u*)(op + 16) = o1;
          }
          if (done >= landed) break;
          ks_tile<11>(w2, slot(done), lane, a < 3);
          lflag_set(FL_P4PART + done * 4 + a, 1);
          ++done;
        }
      }
    }
    if (a == 0 && j == 0 && lane == 0) __hip_atomic_store((GAS u64*)(p.ws + WS_EPOCH + g * 256), tag, __ATOMIC_RELAXED, __HIP_MEMORY_SCOPE_AGENT);
    STAMP(8);
  } else {
    const int b = w - 4;
    const int rot = 2 * xcd;
    const int rho = lane >> 3;
    u32 xv[5];
#pragma unroll
    for (int n = 0; n < 5; ++n) {
      const int i = (5 * b + n) % 10, tau = i >> 1, hf = i & 1;
      const int row = T0 - 16 + 16 * tau + 8 * hf + rho;
      xv[n] = max(row, 0) * 1024 + ((((lane & 7) ^ (rho >> 1)) << 4) ^ (hf * 64)) + 4096 - (n - 1) * 1024;
    }
    const u32 wv = (8 * b + rho) * 3072 + j * 128 + (((((lane & 7) >> 1) ^ (((rho >> 1) & 1) | ((b & 1) << 1))) * 32) + (lane & 1) * 16);
    auto xload = [&](int P) { dma5v(p.x - 4096 + ((2 * P + (b >> 1) + xcd) & 7) * 128, xv, L_X + P * 20480 + (5 * b + 1) * 1024); };
    auto wload = [&](int c) { dma3(p.wqkv + (size_t)((c + rot) & 15) * (32 * 3072), wv, L_RING + (c % NSLOT) * SLOT + b * 3072); };
    const int Tj = 22 * (j >> 1) + ((j & 1) ? (j < 6 ? 12 : 10) : 0);
    const bool full = !(j & 1) && j < 6;
    const int lt = (full || b < 3) ? 3 * b : 8;
    const int nv = (full || b < 2) ? 3 : 2;
    const u32 wev = wcell_voff(lane, j);
    const u32 w1v = (16 * b + ((lane >> 2) & 7)) * 2752 + (Tj + 2 * (lane >> 5) + (((lane >> 1) & 1) ^ (b & 1))) * 32 + (lane & 1) * 16;
    auto w1load = [&](int ks) {
      const char* src = p.w1 - 2048 + (size_t)(64 * (ks >> 1) + 8 * (ks & 1)) * 2752;
      const u32 dst = L_RING + ((ks + 4) % NSLOT) * SLOT + b * 3072;
      dma2v(src, w1v + 2048, w1v + 2048 - 896, dst);
      if (full || lane < 32) dma_more2048(src, w1v + 2048 - 1792);
    };
    auto item = [&](auto I) {
      constexpr int it = decltype(I)::value;
      if constexpr (it >= 24) w1load(it - 24);
      else if constexpr (it >= 20) wcell_dma(p.wo + (size_t)(4 * b + it - 20) * 32768, wev, (it - 20) * 16384 + b * 4096);
      else if constexpr (it >= 16) xload(it - 16);
      else if constexpr (it >= 0) wload(it);
    };
    auto group = [&](auto I) {
      constexpr int gi = decltype(I)::value;
      item(std::integral_constant<int, SCH[gi][0]>{}); item(std::integral_constant<int, SCH[gi][1]>{}); item(std::integral_constant<int, SCH[gi][2]>{});
      item(std::integral_constant<int, SCH[gi][3]>{}); item(std::integral_constant<int, SCH[gi][4]>{});
    };
    STAMP(1);
    group(std::integral_constant<int, 0>{});
    STAMP(7);
    const u32 xo0 = L_X + b * 2048 + (t >> 3) * 1024 + (t & 7) * 128 + ((G ^ ((t >> 1) & 7)) << 4);
    const int t4 = 4 * b + (t & 3);
    const u32 x4 = L_X + ((t >> 3) * 5 + 4) * 2048 + (t4 >> 3) * 1024 + (t4 & 7) * 128 + ((((t >> 2) & 1) * 4 + G) << 4);
    f32x4 s0 = {0, 0, 0, 0}, q0 = {0, 0, 0, 0}, s4 = {0, 0, 0, 0}, q4 = {0, 0, 0, 0};
    const v4u ones = {0x3F803F80u, 0x3F803F80u, 0x3F803F80u, 0x3F803F80u};
    v4u xa_, xb_;
    auto diag = [&](f32x4 v) { const float lo = (t & 1) ? v[1] : v[0], hi = (t & 1) ? v[3] : v[2]; return (t & 2) ? hi : lo; };
    auto step = [&](auto I) {
      constexpr int be = decltype(I)::value;
      vmwait<issued_before_barrier(be) - need_before_barrier(be)>();
      STAMP(12 + be);
      BARRIER();
      group(std::integral_constant<int, be + 1>{});
      constexpr int P = xpair_at(be), base = P == 0 ? 0 : P == 1 ? 3 : P == 2 ? 7 : 10, ph = be - base;
      constexpr int Pp = be > 0 ? xpair_at(be - 1) : 0, bp = Pp == 0 ? 0 : Pp == 1 ? 3 : Pp == 2 ? 7 : 10, php = be > 0 ? be - 1 - bp : 9;
      if constexpr (php < 2) { s0 = mfma32(ones, xa_, s0); q0 = mfma32(xa_, xa_, q0); s0 = mfma32(ones, xb_, s0); q0 = mfma32(xb_, xb_, q0); }
      if constexpr (php == 2) { s4 = mfma32(ones, xa_, s4); q4 = mfma32(xa_, xa_, q4); }
      SB0();
      if constexpr (ph < 2) { xa_ = lds128(xo0 + (2 * P + ph) * 10240); xb_ = lds128((xo0 ^ 64) + (2 * P + ph) * 10240); }
      if constexpr (ph == 2) xa_ = lds128(x4 + P * 20480);
      if constexpr (be == 13) {
        float r, bb;
        ln_coef(s0[0], diag(q0), r, bb);
        if (G == (t >> 2)) { *(LAS float*)(C_R + (16 * b + t) * 4) = r; *(LAS float*)(C_B + (16 * b + t) * 4) = bb; }
      }
      if constexpr (be == 14) {
        float r, bb;
        float sm = s4[0], sq = xg_sum(G == (t >> 2) ? diag(q4) : 0.f);
        sm += dppf<0x128>(sm); sm += dppf<0x124>(sm); sq += dppf<0x128>(sq); sq += dppf<0x124>(sq);
        ln_coef(sm, sq, r, bb);
        if (lane < 4) { *(LAS float*)(C_R + (64 + t4) * 4) = r; *(LAS float*)(C_B + (64 + t4) * 4) = bb; }
      }
    };
    [&]<int... Is>(std::integer_sequence<int, Is...>) { (step(std::integral_constant<int, Is>{}), ...); }(std::make_integer_sequence<int, 16>{});
    BARRIER();
    STAMP(2);
    const u64 tag = *(const LAS u64*)C_TAG;
    v4u w1[16][3];
    {
      wcell_dma(p.w2 + (size_t)(11 * b) * 32768, wev, L_AW + b * 4096);
      w1load(2); w1load(3); w1load(4); w1load(5);
      vmwait<12>();
      lflag_set(FL_WOL + b, 1);
      u32 ra[3];
#pragma unroll
      for (int u = 0; u < 3; ++u) ra[u] = L_RING + G * 3072 + ((lt + u) >> 1) * 512 + ((lane & 15) >> 2) * 64 + ((((lt + u) ^ G) & 1) * 32) + (lane & 3) * 8;
      f32x4 cs[3] = {{0, 0, 0, 0}, {0, 0, 0, 0}, {0, 0, 0, 0}};
      auto w1it = [&](auto I) {
        constexpr int it = decltype(I)::value;
        vmwait<(it < 6 ? 12 : it == 6 ? 6 : 0)>();
        lflag_set(FL_W1L + b, it + 1);
        lwait4(FL_W1L, it + 1);
#pragma unroll
        for (int k2 = 0; k2 < 2; ++k2)
#pragma unroll
          for (int u = 0; u < 3; ++u) {
            const u32 ad = ra[u] + ((2 * it + k2 + 4) % NSLOT) * SLOT;
            v2u lo = ldstr(ad), hi = ldstr(ad + 256); w1[2 * it + k2][u] = v4u{lo.x, lo.y, hi.x, hi.y};
          }
        asm volatile("s_waitcnt lgkmcnt(0)" ::: "memory");
        lflag_set(FL_W1C + b, it + 1);
        if constexpr (it < 5) { lwait4(FL_W1C, it + 1); w1load(2 * it + 6); w1load(2 * it + 7); }
#pragma unroll
        for (int k2 = 0; k2 < 2; ++k2)
#pragma unroll
          for (int u = 0; u < 3; ++u) cs[u] = mfma32(w1[2 * it + k2][u], ones, cs[u]);
      };
      [&]<int... Is>(std::integer_sequence<int, Is...>) { (w1it(std::integral_constant<int, Is>{}), ...); }(std::make_integer_sequence<int, 8>{});
      if (t == 0) {
#pragma unroll
        for (int u = 0; u < 3; ++u) *(LAS f32x4*)(C_CS1 + (b * 3 + u) * 64 + G * 16) = cs[u];
      }
    }
    STAMP(3);
    const u32 fco = WS_FLC + (g * 4 + (j >> 1)) * 256 + ((j & 1) * 4 + b) * 8;
    int issued = 0, marked = 0;
    auto hservice = [&]() {
      const int landed = issued;
      u32 rdy = 0;
      if (issued < 4) { const u32 m = (u32)gflag_poll(p.ws, WS_FLB + (g * 4 + b) * 256, tag, lane, 8); rdy = m & (m >> 4); }
      VMWAIT0();
      while (issued < 4 && ((rdy >> issued) & 1)) {
        lwait1(FL_P2FIN + issued, 1);
        dma4_sc1(p.ws + WS_H + ((size_t)(g * 4 + issued) * 16 + 4 * b) * 1024, lane * 16, issued * 16384 + b * 4096);
        ++issued;
      }
      for (; marked < landed; ++marked) lflag_set(FL_HRDY + marked * 4 + b, 1);
    };
#pragma unroll 1
    for (int i = 0; i < 4; ++i) {
      for (u32 s = 0; s < SPIN_G && marked <= i; ++s) hservice();
      lwait4(FL_HRDY + i * 4, 1);
      if (i == 0) STAMP(4);
      f32x4 acc[3] = {{0, 0, 0, 0}, {0, 0, 0, 0}, {0, 0, 0, 0}};
      const u32 ha = i * 16384 + lane * 16;
      constexpr int PD = 5;
      v4u hf[16];
      SB0();
#pragma unroll
      for (int ks = 0; ks < PD; ++ks) hf[ks] = lds128(ha + ks * 1024);
      SB0();
#pragma unroll
      for (int ks = 0; ks < 16; ++ks) {
        acc[0] = mfma32(w1[ks][0], hf[ks], acc[0]);
        SB0();
        if (ks + PD < 16) hf[ks + PD] = lds128(ha + (ks + PD) * 1024);
        SB0();
        acc[1] = mfma32(w1[ks][1], hf[ks], acc[1]);
        acc[2] = mfma32(w1[ks][2], hf[ks], acc[2]);
        SB0();
      }
      if (i == 0) STAMP(8);
      if (marked < 4) hservice();
      if (i > 0) gflag_pub(p.ws, fco + (i - 1) * 64, tag, lane);
      if (i == 1) STAMP(9);
      lwait4(FL_SRDY + i * 4, 1);
      const f32x4 sm = ldsf4(C_S2 + (i * 16 + t) * 16), sq = ldsf4(C_S2 + 1024 + (i * 16 + t) * 16);
      float r, bb;
      ln_coef((sm[0] + sm[1]) + (sm[2] + sm[3]), (sq[0] + sq[1]) + (sq[2] + sq[3]), r, bb);
#pragma unroll
      for (int u = 0; u < 3; ++u) {
        if (u == 2 && nv < 3) continue;
        const f32x4 c4 = ldsf4(C_CS1 + (b * 3 + u) * 64 + G * 16);
        f32x4 v = acc[u] * r + c4 * bb;
        const v2u fo = {pk2(fmaxf(v[0], 0.f), fmaxf(v[1], 0.f)), pk2(fmaxf(v[2], 0.f), fmaxf(v[3], 0.f))};
        __builtin_amdgcn_raw_buffer_store_b64(fo, rws, lane * 8, WS_F + (g * 4 + i) * F_TAU + (Tj + lt + u) * 512, 16);
      }
      asm volatile("s_waitcnt lgkmcnt(0)" ::: "memory");
      lflag_set(FL_BDONE + b, i + 1);
    }
    STAMP(5);
    gflag_pub(p.ws, fco + 3 * 64, tag, lane);
    STAMP(6);
  }
  STAMP_OUT();
}
}

extern "C" void kernel_launch(void* const* d_in, const int* in_sizes, int n_in, void* d_out, int out_size, void* d_ws, size_t ws_size,
                              hipStream_t stream) {
  fb::Params p;
  p.x = (const char*)d_in[0]; p.wqkv = (const char*)d_in[1]; p.wo = (const char*)d_in[2];
  p.w1 = (const char*)d_in[4]; p.w2 = (const char*)d_in[5];
  p.out = (char*)d_out; p.ws = (char*)d_ws;
  (void)hipFuncSetAttribute((const void*)fb::fused_block, hipFuncAttributeMaxDynamicSharedMemorySize, fb::L_TOTAL);
  fb::fused_block<<<256, 512, fb::L_TOTAL, stream>>>(p);
}
```

```cpp
#include <hip/hip_runtime.h>
#include <hip/hip_bf16.h>
#include <utility>
#pragma clang diagnostic ignored "-Wint-to-pointer-cast"
#pragma clang diagnostic ignored "-Wc++20-extensions"

#define NOSTATS 0
#define NOMMA 0
#define P1ONLY 0
#define MAPC 2
namespace fb {
typedef unsigned u32; typedef unsigned long long u64; typedef unsigned short u16;
typedef u32 v4u __attribute__((ext_vector_type(4)));
typedef u32 v2u __attribute__((ext_vector_type(2)));
typedef float f32x4 __attribute__((ext_vector_type(4)));
typedef float f32x2 __attribute__((ext_vector_type(2)));
typedef __bf16 bf8 __attribute__((ext_vector_type(8)));
typedef __bf16 bf2 __attribute__((ext_vector_type(2)));
typedef short s4 __attribute__((ext_vector_type(4)));
#define LAS __attribute__((address_space(3)))
#define GAS __attribute__((address_space(1)))
#define DEV __device__ __forceinline__

constexpr u32 G_EPOCH = 0;
constexpr u32 G_FLA = 1024, G_FLB = 2048, G_FLC = 3072;
constexpr u32 G_ST = 4096;
constexpr u32 G_O = 8192;
constexpr u32 G_H = G_O + 65536;
constexpr u32 G_F = G_H + 65536;
constexpr u32 F_TAU = 86 * 512, G_BYTES = 327680;
__host__ __device__ constexpr size_t group_base(int g) { return (size_t)(g >> 2) * (2u << 20) + (size_t)(g & 3) * G_BYTES; }
constexpr u32 WS_DBG = 16u << 20;
constexpr u32 L_X = 0, L_RING = 81920, SLOT = 12288, NSLOT = 6, L_CTRL = 155648, L_TOTAL = 163840;
constexpr u32 L_AW = 65536, AWP = 22528, FPART = 11264;
constexpr u32 C_R = L_CTRL, C_B = C_R + 320, C_TAG = C_B + 320;
constexpr u32 C_FLAGS = L_CTRL + 1024;
constexpr u32 FL_P2PART = 0, FL_P2FIN = 16, FL_HRDY = 32, FL_WOL = 64, FL_BDONE = 68, FL_W1L = 72, FL_W1C = 76, FL_P4PART = 80, FL_P4FIN = 96;
constexpr u32 C_CS1 = L_CTRL + 2048;
constexpr u32 C_TB = L_CTRL + 1536;
constexpr u32 C_S2 = L_CTRL + 4096;
constexpr u32 SPIN_G = 1u << 16, SPIN_L = 1u << 20;

struct Params { const char* x; const char* wqkv; const char* wo; const char* w1; const char* w2; char* out; char* ws; };

DEV f32x4 mfma32(v4u a, v4u b, f32x4 c) { return __builtin_amdgcn_mfma_f32_16x16x32_bf16(__builtin_bit_cast(bf8, a), __builtin_bit_cast(bf8, b), c, 0, 0, 0); }
DEV f32x4 mfma16(v2u a, v2u b, f32x4 c) { return __builtin_amdgcn_mfma_f32_16x16x16bf16_1k(__builtin_bit_cast(s4, a), __builtin_bit_cast(s4, b), c, 0, 0, 0); }
DEV void mfma32t(f32x4& c, v4u a, v4u b) { asm volatile("v_mfma_f32_16x16x32_bf16 %0, %1, %2, %0" : "+v"(c) : "v"(a), "v"(b)); }
DEV void mfma32z(f32x4& c, v4u a, v4u b) { asm volatile("v_mfma_f32_16x16x32_bf16 %0, %1, %2, 0" : "=&v"(c) : "v"(a), "v"(b)); }
DEV u32 pk2(float a, float b) { bf2 p = {(__bf16)a, (__bf16)b}; return __builtin_bit_cast(u32, p); }
DEV v2u pk4(f32x4 v) { return v2u{pk2(v[0], v[1]), pk2(v[2], v[3])}; }
DEV float asf(u32 u) { return __builtin_bit_cast(float, u); }
DEV u32 asu(float f) { return __builtin_bit_cast(u32, f); }
DEV f32x4 unpk4(u32 lo, u32 hi) { return f32x4{asf(lo << 16), asf(lo & 0xffff0000u), asf(hi << 16), asf(hi & 0xffff0000u)}; }
DEV u32 uni(u32 v) { return __builtin_amdgcn_readfirstlane(v); }
DEV v4u lds128(u32 a) { return *(const LAS v4u*)a; }
DEV v2u lds64(u32 a) { return *(const LAS v2u*)a; }
DEV f32x4 ldsf4(u32 a) { return *(const LAS f32x4*)a; }
DEV f32x2 ldsf2(u32 a) { return *(const LAS f32x2*)a; }
DEV float ldsf(u32 a) { return *(const LAS float*)a; }
DEV v2u ldstr(u32 a) { return __builtin_bit_cast(v2u, __builtin_amdgcn_ds_read_tr16_b64_v4i16((LAS s4*)a)); }
DEV float vmax(float a, float b) { float r; asm("v_max_f32 %0, %1, %2" : "=v"(r) : "v"(a), "v"(b)); return r; }
#define MEMFENCE() asm volatile("" ::: "memory")
#define SB0() __builtin_amdgcn_sched_barrier(0)
#define BARRIER() do { SB0(); asm volatile("s_waitcnt lgkmcnt(0)\n\ts_barrier" ::: "memory"); SB0(); } while (0)
#define SGB(m, n) __builtin_amdgcn_sched_group_barrier(m, n, 0)
#define VMWAIT0() asm volatile("s_waitcnt vmcnt(0)" ::: "memory")
#define LGWAIT0() asm volatile("s_waitcnt lgkmcnt(0)" ::: "memory")
template <int N> DEV void vmwait() { asm volatile("s_waitcnt vmcnt(%0)" ::"i"(N) : "memory"); }
#define GLD "global_load_lds_dwordx4 "
DEV void dma3(const char* sb, u32 v, u32 dst) {
  asm volatile("s_mov_b32 m0, %2\n\ts_nop 0\n\t" GLD "%0, %1\n\t" GLD "%0, %1 offset:1024\n\t" GLD "%0, %1 offset:2048" ::"v"(v), "s"(sb), "s"(dst) : "memory");
}
DEV void dma2v(const char* sb, u32 v0, u32 v1, u32 dst) {
  asm volatile("s_mov_b32 m0, %3\n\ts_nop 0\n\t" GLD "%0, %2\n\t" GLD "%1, %2 offset:1024" ::"v"(v0), "v"(v1), "s"(sb), "s"(dst) : "memory");
}
DEV void dma3v(const char* sb, u32 v0, u32 v1, u32 v2, u32 dst) {
  asm volatile("s_mov_b32 m0, %4\n\ts_nop 0\n\t" GLD "%0, %3\n\t" GLD "%1, %3 offset:1024\n\t" GLD "%2, %3 offset:2048"
               ::"v"(v0), "v"(v1), "v"(v2), "s"(sb), "s"(dst) : "memory");
}
DEV void dma4v(const char* sb, u32 v0, u32 v1, u32 v2, u32 v3, u32 dst) {
  asm volatile("s_mov_b32 m0, %5\n\ts_nop 0\n\t" GLD "%0, %4\n\t" GLD "%1, %4 offset:1024\n\t" GLD "%2, %4 offset:2048\n\t" GLD "%3, %4 offset:3072"
               ::"v"(v0), "v"(v1), "v"(v2), "v"(v3), "s"(sb), "s"(dst) : "memory");
}
DEV void dma4w(const char* sb, u32 v0, u32 v1, u32 v2, u32 v3, u32 dst) {
  asm volatile("s_mov_b32 m0, %5\n\ts_nop 0\n\t" GLD "%0, %4\n\t" GLD "%1, %4 offset:1152\n\t" GLD "%2, %4 offset:2048\n\t" GLD "%3, %4 offset:3200"
               ::"v"(v0), "v"(v1), "v"(v2), "v"(v3), "s"(sb), "s"(dst) : "memory");
}
DEV void dma1_sc1(const char* sb, u32 v, u32 dst) {
  asm volatile("s_mov_b32 m0, %2\n\ts_nop 0\n\t" GLD "%0, %1 sc1" ::"v"(v), "s"(sb), "s"(dst) : "memory");
}
DEV void dma4_sc1(const char* sb, u32 v, u32 dst) {
  asm volatile("s_mov_b32 m0, %2\n\ts_nop 0\n\t" GLD "%0, %1 sc1\n\t" GLD "%0, %1 offset:1024 sc1\n\t" GLD "%0, %1 offset:2048 sc1\n\t"
               GLD "%0, %1 offset:3072 sc1" ::"v"(v), "s"(sb), "s"(dst) : "memory");
}
DEV void dma8_sc1(const char* sb, u32 v, u32 dst) {
  asm volatile("s_mov_b32 m0, %2\n\ts_nop 0\n\t" GLD "%0, %1 offset:-4096 sc1\n\t" GLD "%0, %1 offset:-3072 sc1\n\t" GLD "%0, %1 offset:-2048 sc1\n\t"
               GLD "%0, %1 offset:-1024 sc1\n\t" GLD "%0, %1 sc1\n\t" GLD "%0, %1 offset:1024 sc1\n\t" GLD "%0, %1 offset:2048 sc1\n\t"
               GLD "%0, %1 offset:3072 sc1" ::"v"(v), "s"(sb), "s"(dst) : "memory");
}
DEV void dma2_sc1(const char* sb, u32 v, u32 dst) {
  asm volatile("s_mov_b32 m0, %2\n\ts_nop 0\n\t" GLD "%0, %1 sc1\n\t" GLD "%0, %1 offset:1024 sc1" ::"v"(v), "s"(sb), "s"(dst) : "memory");
}
DEV void dma_more2048_sc1(const char* sb, u32 v) { asm volatile(GLD "%0, %1 offset:2048 sc1" ::"v"(v), "s"(sb) : "memory"); }
DEV void lflag_set(u32 word, u32 v) {
  MEMFENCE();
  __hip_atomic_store((LAS u32*)(C_FLAGS + word * 4), v, __ATOMIC_RELAXED, __HIP_MEMORY_SCOPE_WORKGROUP);
  MEMFENCE();
}
DEV u32 lflag_min4(u32 word) {
  v4u v;
  asm volatile("ds_read_b128 %0, %1\n\ts_waitcnt lgkmcnt(0)" : "=v"(v) : "v"(C_FLAGS + word * 4) : "memory");
  u32 m = min(min(v.x, v.y), min(v.z, v.w));
  return uni(m);
}
DEV u32 lflag_get(u32 word) {
  u32 v;
  asm volatile("ds_read_b32 %0, %1\n\ts_waitcnt lgkmcnt(0)" : "=v"(v) : "v"(C_FLAGS + word * 4) : "memory");
  return uni(v);
}
DEV void lwait4(u32 word, u32 want) { for (u32 s = 0; s < SPIN_L && lflag_min4(word) < want; ++s) {} }
DEV void lwait1(u32 word, u32 want) { for (u32 s = 0; s < SPIN_L && lflag_get(word) < want; ++s) {} }
DEV void gflag_pub(char* gw, u32 off, u64 tag, int lane) {
  VMWAIT0();
  if (lane == 0) __hip_atomic_store((GAS u64*)(gw + off), tag, __ATOMIC_RELAXED, __HIP_MEMORY_SCOPE_AGENT);
  MEMFENCE();
}
DEV u64 gflag_poll(char* gw, u32 off, u64 tag, int lane, int n) {
  u64 v = __hip_atomic_load((GAS u64*)(gw + off + (lane & (n - 1)) * 8), __ATOMIC_RELAXED, __HIP_MEMORY_SCOPE_AGENT);
  u64 m = __builtin_amdgcn_ballot_w64(v == tag);
  MEMFENCE();
  return m;
}
DEV u64 mix64(u64 z) {
  z += 0x9E3779B97F4A7C15ull; z = (z ^ (z >> 30)) * 0xBF58476D1CE4E5B9ull; z = (z ^ (z >> 27)) * 0x94D049BB133111EBull; return z ^ (z >> 31);
}
DEV float xg_sum(float v) {
  auto a = __builtin_amdgcn_permlane16_swap(asu(v), asu(v), false, false);
  v = asf(a[0]) + asf(a[1]);
  auto b = __builtin_amdgcn_permlane32_swap(asu(v), asu(v), false, false);
  return asf(b[0]) + asf(b[1]);
}
template <int CTRL> DEV float dppf(float v) { return asf(__builtin_amdgcn_update_dpp(0u, asu(v), CTRL, 0xf, 0xf, false)); }
DEV void ln_coef(float s, float q, float& r, float& b) {
  float m = s * (1.0f / 512.0f);
  float var = fmaxf(q * (1.0f / 512.0f) - m * m, 0.0f);
  r = __builtin_amdgcn_rsqf(var + 1e-5f);
  b = -m * r;
}
#define STAMP(i)
#define CSTAMP(i)
#define STAMP_DECL
#define STAMP_OUT()

struct Frag4 { v4u f[4]; };
DEV void wcell_dma(const char* src, u32 ve, u32 dst) { dma4v(src, ve, (ve ^ 64) + 3072, ve + 6144, (ve ^ 64) + 9216, dst); }
DEV u32 wcell_voff(int lane, int j) {
  const int r = lane >> 3;
  return (16 * (r >> 2) + (r & 3)) * 1024 + j * 128 + ((((lane & 7) >> 1) ^ ((r >> 1) & 1)) * 32) + (lane & 1) * 16;
}
DEV u32 wcell_roff(int lane) {
  const int G = lane >> 4, q = (lane & 15) >> 2;
  return G * 1024 + q * 128 + ((((q >> 1) & 1) | ((G & 1) << 1)) * 32) + (lane & 3) * 8;
}
DEV void wcell_read(Frag4& Fg, u32 cell, u32 ro) {
#pragma unroll
  for (int c = 0; c < 4; ++c) { const u32 ad = cell + (ro ^ (c * 32)); v2u lo = ldstr(ad), hi = ldstr(ad + 512); Fg.f[c] = v4u{lo.x, lo.y, hi.x, hi.y}; }
}
DEV v4u act_frag(u32 a) { v2u lo = lds64(a), hi = lds64(a + 512); return v4u{lo.x, lo.y, hi.x, hi.y}; }

constexpr int SCH[17][4] = {{1, 2, 3, 4},     {5, 18, -1, -1},  {6, 19, -1, -1},  {7, 20, -1, -1},  {8, 21, -1, -1},  {9, 22, -1, -1},
                            {10, 23, -1, -1}, {11, -1, -1, -1}, {12, -1, -1, -1}, {13, -1, -1, -1}, {14, -1, -1, -1}, {15, -1, -1, -1},
                            {24, -1, -1, -1}, {25, -1, -1, -1}, {26, -1, -1, -1}, {27, -1, -1, -1}, {-1, -1, -1, -1}};
constexpr int item_ops(int it) { return it < 0 ? 0 : it < 16 ? 3 : it < 24 ? ((it & 1) ? 2 : 3) : 4; }
constexpr int item_end(int it) { int n = 0; for (int gI = 0; gI < 17; ++gI) for (int k = 0; k < 4; ++k) { n += item_ops(SCH[gI][k]); if (SCH[gI][k] == it) return n; } return 0; }
constexpr int issued_before_barrier(int b) { int n = 0; for (int gI = 0; gI <= b; ++gI) for (int k = 0; k < 4; ++k) n += item_ops(SCH[gI][k]); return n; }
constexpr int xpair_at(int b) { return b < 4 ? 0 : b < 7 ? 1 : b < 9 ? 2 : 3; }
constexpr int need_before_barrier(int b) { return item_end(b) > item_end(17 + 2 * xpair_at(b)) ? item_end(b) : item_end(17 + 2 * xpair_at(b)); }

__global__ void __launch_bounds__(512, 2) fused_block(Params p) {
  extern __shared__ __attribute__((aligned(16))) char smem[];
  const int tid = threadIdx.x, lane = tid & 63;
  const int w = uni(tid >> 6);
  const int bid = blockIdx.x, xcd = bid & 7, idx = bid >> 3;
  const int g = (xcd >> 1) * 8 + (idx >> 2), j = (xcd & 1) * 4 + (idx & 3);
  const int T0 = g * 64;
  const int t = lane & 15, G = lane >> 4;
  char* const gw = p.ws + group_base(g);
  const __amdgpu_buffer_rsrc_t rws = __builtin_amdgcn_make_buffer_rsrc((void*)gw, 0, G_BYTES, 0x00020000);
  STAMP_DECL;
  STAMP(0);
  asm volatile("" ::"s"(smem));
  const int b = w & 3, rho = lane >> 3;
  const u32 wv = (8 * b + rho) * 3072 + j * 128 + (((((lane & 7) >> 1) ^ (((rho >> 1) & 1) | ((b & 1) << 1))) * 32) + (lane & 1) * 16);
  auto wload = [&](int c) { dma3(p.wqkv + (size_t)((c + 2 * xcd) & 15) * (32 * 3072), wv, L_RING + (c % NSLOT) * SLOT + b * 3072); };
  if (w < 4) wload(0); else { wload(1); wload(2); wload(3); wload(4); }
  u32 xv[5];
#pragma unroll
  for (int n = 0; n < 5; ++n) {
    const int i = (5 * b + n) % 10, tau = i >> 1, hf = i & 1;
    const int row = T0 - 16 + 16 * tau + 8 * hf + rho;
    xv[n] = max(row, 0) * 1024 + ((((lane & 7) ^ (rho >> 1)) << 4) ^ (hf * 64)) + 2048 - (n < 3 ? n : n - 3) * 1024;
  }
  auto xload = [&](int P, int part) {
    const char* src = p.x - 2048 + ((2 * P + (b >> 1) + xcd) & 7) * 128;
    if (part == 0) dma3v(src, xv[0], xv[1], xv[2], L_X + P * 20480 + 5 * b * 1024);
    else dma2v(src, xv[3], xv[4], L_X + P * 20480 + (5 * b + 3) * 1024);
  };
  if (w < 4) { xload(0, 0); xload(0, 1); }
  const v4u ones = {0x3F803F80u, 0x3F803F80u, 0x3F803F80u, 0x3F803F80u};
  const f32x4 zero4 = {0, 0, 0, 0};

  int wr_ = w;
  asm volatile("" : "+s"(wr_));
  if (wr_ < 4) {
    const int a = w;
    __builtin_amdgcn_s_setprio(2);
    if (a == 0) { *(LAS u32*)(C_FLAGS + lane * 4) = 0; *(LAS u32*)(C_FLAGS + 256 + lane * 4) = 0; }
    f32x4 aq[5], ak[5], av[5];
#pragma unroll
    for (int i = 0; i < 5; ++i) { aq[i] = zero4; ak[i] = zero4; av[i] = zero4; }
    const int q = (lane & 15) >> 2, pp = lane & 3;
    const u32 woff = L_RING + G * 3072 + q * 128 + ((a ^ (((q >> 1) & 1) | ((G & 1) << 1))) * 32) + pp * 8;
    const u32 xo0 = L_X + (t >> 3) * 1024 + (t & 7) * 128 + ((G ^ ((t >> 1) & 7)) << 4);
    struct Fr { v4u w[3]; v4u x[5]; };
    auto ldfr = [&](Fr& f, u32 wa, u32 xa) {
#pragma unroll
      for (int m = 0; m < 3; ++m) { v2u lo = ldstr(wa + m * 1024), hi = ldstr(wa + m * 1024 + 512); f.w[m] = v4u{lo.x, lo.y, hi.x, hi.y}; }
#pragma unroll
      for (int i = 0; i < 5; ++i) f.x[i] = lds128(xa + i * 2048);
    };
    auto mma = [&](Fr& f) {
      if (NOMMA) { for (int i = 0; i < 5; ++i) asm volatile("" :: "v"(f.x[i])); for (int i = 0; i < 3; ++i) asm volatile("" :: "v"(f.w[i])); return; }
      v4u x0 = f.x[0];
      if (t == 0) x0 = ones;
#pragma unroll
      for (int i = 1; i < 5; ++i) { aq[i] = mfma32(f.w[0], f.x[i], aq[i]); ak[i] = mfma32(f.w[1], f.x[i], ak[i]); av[i] = mfma32(f.x[i], f.w[2], av[i]); }
      aq[0] = mfma32(f.w[0], x0, aq[0]); ak[0] = mfma32(f.w[1], x0, ak[0]); av[0] = mfma32(x0, f.w[2], av[0]);
    };
    Fr f0, f1;
    u32 so = 0, xa = xo0;
#pragma unroll
    for (int i = 0; i < 5; ++i) asm volatile("" : "+v"(aq[i]), "+v"(ak[i]), "+v"(av[i]));
    VMWAIT0();
    BARRIER();
    STAMP(1); CSTAMP(24);
    ldfr(f0, woff, xa);
    u32 zz = 0; asm volatile("" : "+v"(zz));
    const u64 epv = __hip_atomic_load((GAS u64*)(gw + G_EPOCH + zz), __ATOMIC_RELAXED, __HIP_MEMORY_SCOPE_AGENT);
    v2u xres[4];
    const u32 xra = L_X + (((j - xcd) & 7) * 5 + a + 1) * 2048 + (t >> 3) * 1024 + (t & 7) * 128 + (((G >> 1) ^ ((t >> 1) & 7)) << 4) + (G & 1) * 8;
    auto pat = [&]() {
#pragma unroll
      for (int i = 0; i < 6; ++i) { SGB(0x8, 1); SGB(0x100, 2); }
      SGB(0x8, 9);
    };
#pragma unroll 1
    for (int it = 0; it < 7; ++it) {
      so = (so == (NSLOT - 1) * SLOT) ? 0 : so + SLOT;
      BARRIER();
      ldfr(f1, woff + so, xa ^ 64);
      mma(f0);
      pat();
      so = (so == (NSLOT - 1) * SLOT) ? 0 : so + SLOT;
      xa += 10240;
      BARRIER();
      ldfr(f0, woff + so, xa);
      mma(f1);
      pat();
      SB0();
      if (it <= 4) {
#pragma unroll
        for (int c = 0; c < 4; ++c) xres[c] = lds64(xra ^ (c * 32));
      }
    }
    BARRIER();
    ldfr(f1, woff + 3 * SLOT, xa ^ 64);
    mma(f0);
    pat();
    SB0();
    const u64 tag = mix64(((u64)uni((u32)(epv >> 32)) << 32) | uni((u32)epv));
    if (a == 0 && lane == 0) *(LAS u64*)C_TAG = tag;
    float rt[5], bt[5]; f32x4 r4[5], b4[5];
#pragma unroll
    for (int i = 0; i < 5; ++i) {
      rt[i] = ldsf(C_R + (16 * i + t) * 4); bt[i] = ldsf(C_B + (16 * i + t) * 4);
      r4[i] = ldsf4(C_R + (16 * i + 4 * G) * 4); b4[i] = ldsf4(C_B + (16 * i + 4 * G) * 4);
    }
    BARRIER();
    {
      v4u x0 = f1.x[0];
      if (t == 0) x0 = ones;
      aq[0] = mfma32(f1.w[0], x0, aq[0]); ak[0] = mfma32(f1.w[1], x0, ak[0]); av[0] = mfma32(x0, f1.w[2], av[0]);
#pragma unroll
      for (int i = 1; i < 5; ++i) { ak[i] = mfma32(f1.w[1], f1.x[i], ak[i]); aq[i] = mfma32(f1.w[0], f1.x[i], aq[i]); }
#pragma unroll
      for (int i = 1; i < 5; ++i) av[i] = mfma32(f1.x[i], f1.w[2], av[i]);
    }
    STAMP(2); CSTAMP(25);
    {
      v2u kb[5], vb[5], qb[5];
      f32x4 csq, csk;
#pragma unroll
      for (int r = 0; r < 4; ++r) { csq[r] = dppf<0x150>(aq[0][r]); csk[r] = dppf<0x150>(ak[0][r]); }
      const float csv = __shfl(av[0][0], t);
#pragma unroll
      for (int i = 0; i < 5; ++i) {
        kb[i] = pk4(ak[i] * rt[i] + csk * bt[i]);
        if (i > 0) qb[i] = pk4(aq[i] * rt[i] + csq * bt[i]);
      }
      SB0();
      f32x4 sp[4], sc[4];
#pragma unroll
      for (int i = 0; i < 4; ++i) { sp[i] = mfma16(kb[i], qb[i + 1], zero4); sc[i] = mfma16(kb[i + 1], qb[i + 1], zero4); }
#pragma unroll
      for (int i = 0; i < 5; ++i) vb[i] = pk4(av[i] * r4[i] + csv * b4[i]);
      SB0();
      const bool par = t & 1;
      const bool cur0 = 4 * G + (t & 1) <= t, cur1 = 4 * G + (t & 1) + 2 <= t;
      const u32 sh = (t & 1) * 16;
      float z0[4], z1[4], m[4];
#pragma unroll
      for (int i = 0; i < 4; ++i) {
        const float c0 = par ? sc[i][1] : sc[i][0], c1 = par ? sc[i][3] : sc[i][2];
        float p0 = par ? sp[i][1] : sp[i][0], p1 = par ? sp[i][3] : sp[i][2];
        if (i == 0 && g == 0) { p0 = -4e9f; p1 = -4e9f; }
        z0[i] = cur0 ? c0 : p0; z1[i] = cur1 ? c1 : p1;
        m[i] = vmax(z0[i], z1[i]);
      }
      SB0();
#pragma unroll
      for (int i = 0; i < 4; ++i) { auto s_ = __builtin_amdgcn_permlane16_swap(asu(m[i]), asu(m[i]), false, false); m[i] = vmax(asf(s_[0]), asf(s_[1])); }
      SB0();
#pragma unroll
      for (int i = 0; i < 4; ++i) { auto s_ = __builtin_amdgcn_permlane32_swap(asu(m[i]), asu(m[i]), false, false); m[i] = vmax(asf(s_[0]), asf(s_[1])); }
      SB0();
      f32x4 o[4], den[4];
#pragma unroll
      for (int i = 0; i < 4; ++i) {
        const float e0 = __builtin_amdgcn_exp2f((z0[i] - m[i]) * 0.36067376022f), e1 = __builtin_amdgcn_exp2f((z1[i] - m[i]) * 0.36067376022f);
        const u32 u = pk2(e0, e1);
        const u32 w0 = (u & 0xffffu) << sh, w1 = (u >> 16) << sh;
        const v4u P = {cur0 ? 0u : w0, cur1 ? 0u : w1, cur0 ? w0 : 0u, cur1 ? w1 : 0u};
        const v4u V = {vb[i].x, vb[i].y, vb[i + 1].x, vb[i + 1].y};
        den[i] = mfma32(ones, P, zero4);
        o[i] = mfma32(V, P, zero4);
      }
      SB0();
#pragma unroll
      for (int i = 0; i < 4; ++i) {
        const v2u ob = pk4(o[i] * __builtin_amdgcn_rcpf(den[i][0]));
        __builtin_amdgcn_raw_buffer_store_b64(ob, rws, lane * 8, G_O + (i * 32 + 4 * j + a) * 512, 16);
      }
    }
    STAMP(23);
    if (P1ONLY) { STAMP(8); STAMP_OUT(); __builtin_amdgcn_endpgm(); }
    const u32 ro = wcell_roff(lane);
    Frag4 w2[11];
    v4u hres0, hres1;
    {
      Frag4 wo[4];
      lwait1(FL_WOL + a, 1);
#pragma unroll
      for (int s = 0; s < 4; ++s) wcell_read(wo[s], s * 16384 + a * 4096, ro);
      gflag_pub(gw, G_FLA + (j >> 1) * 256 + ((j & 1) * 4 + a) * 8, tag, lane);
      STAMP(3);
      LGWAIT0();
      lflag_set(FL_P2PART + a * 4 + a, 1);
      for (u32 s = 0; s < SPIN_G && (gflag_poll(gw, G_FLA + a * 256, tag, lane, 8) & 0xff) != 0xff; ++s) {}
      STAMP(4);
      u32 cell[4];
#pragma unroll
      for (int i = 0; i < 4; ++i) {
        const int tl = (a + 1 + i) & 3;
        cell[i] = tl * 16384 + a * 4096;
        dma4_sc1(gw + G_O + (tl * 32 + 8 * a) * 512, lane * 16, cell[i]);
      }
      v4u bf[4][4];
      f32x4 acc[4][4];
      auto rd = [&](int i) {
#pragma unroll
        for (int s = 0; s < 4; ++s) bf[i][s] = act_frag(cell[i] + lane * 8 + s * 1024);
      };
      auto mm = [&](int i) {
#pragma unroll
        for (int s = 0; s < 4; ++s)
#pragma unroll
          for (int c = 0; c < 4; ++c) acc[i][c] = mfma32(wo[s].f[c], bf[i][s], (s == 0 && i < 3) ? zero4 : acc[i][c]);
      };
      auto wr = [&](int i) {
#pragma unroll
        for (int c = 0; c < 4; ++c) *(LAS f32x4*)(cell[i] + c * 1024 + lane * 16) = acc[i][c];
        lflag_set(FL_P2PART + ((a + 1 + i) & 3) * 4 + a, 1);
      };
#pragma unroll
      for (int c = 0; c < 4; ++c) acc[3][c] = unpk4(xres[c].x, xres[c].y);
      vmwait<12>(); rd(0);
      STAMP(9);
      vmwait<8>(); rd(1); SB0();
      mm(0); SB0();
      vmwait<4>(); rd(2); SB0();
      mm(1); wr(0); SB0();
      vmwait<0>(); rd(3); SB0();
      mm(2); wr(1); SB0();
      wr(2); SB0();
      mm(3); SB0();
      STAMP(16);
      lwait4(FL_P2PART + a * 4, 1);
      STAMP(17);
      {
        const u32 pb = a * 16384 + lane * 16;
        const u32 o1 = ((a + 1) & 3) * 4096, o2 = ((a + 2) & 3) * 4096, o3 = ((a + 3) & 3) * 4096;
        f32x4 pv[4];
#pragma unroll
        for (int c = 0; c < 4; ++c) pv[c] = (ldsf4(pb + o1 + c * 1024) + ldsf4(pb + o2 + c * 1024)) + ldsf4(pb + o3 + c * 1024);
#pragma unroll
        for (int c = 0; c < 4; ++c) acc[3][c] += pv[c];
      }
      const v2u h0 = pk4(acc[3][0]), h1 = pk4(acc[3][1]), h2 = pk4(acc[3][2]), h3 = pk4(acc[3][3]);
      hres0 = v4u{h0.x, h0.y, h1.x, h1.y}; hres1 = v4u{h2.x, h2.y, h3.x, h3.y};
      const u32 ho = G_H + (a * 16 + 2 * j) * 1024;
      __builtin_amdgcn_raw_buffer_store_b128(hres0, rws, lane * 16, ho, 16);
      __builtin_amdgcn_raw_buffer_store_b128(hres1, rws, lane * 16 + 1024, ho, 16);
      SB0();
      lflag_set(FL_P2FIN + a, 1);
      {
        const f32x4 s4 = (acc[3][0] + acc[3][1]) + (acc[3][2] + acc[3][3]);
        const f32x4 q4 = (acc[3][0] * acc[3][0] + acc[3][1] * acc[3][1]) + (acc[3][2] * acc[3][2] + acc[3][3] * acc[3][3]);
        const float sm = xg_sum((s4[0] + s4[1]) + (s4[2] + s4[3])), sq = xg_sum((q4[0] + q4[1]) + (q4[2] + q4[3]));
        if (lane < 16) __builtin_amdgcn_raw_buffer_store_b64(v2u{asu(sm), asu(sq)}, rws, lane * 8, G_ST + (j >> 1) * 1024 + (a * 2 + (j & 1)) * 128, 16);
      }
      STAMP(12);
      gflag_pub(gw, G_FLB + (j >> 1) * 256 + ((j & 1) * 4 + a) * 8, tag, lane);
    }
    STAMP(5);
    __builtin_amdgcn_s_setprio(0);
    {
      const u32 aw = L_AW + a * AWP;
      const u32 ve = wcell_voff(lane, j);
      const char* w2s = p.w2 + (size_t)(11 * a) * 32768;
      lwait1(FL_W1C + a, 1);
#pragma unroll
      for (int s = 0; s < 5; ++s) wcell_read(w2[s], aw + s * 4096, ro);
      STAMP(13);
      lwait4(FL_HRDY, 2);
      STAMP(6);
#pragma unroll
      for (int s = 5; s < 10; ++s) wcell_dma(w2s + s * 32768, ve, aw + (s - 5) * 4096);
      vmwait<16>();
      wcell_read(w2[5], aw, ro);
      LGWAIT0();
      wcell_dma(w2s + (a == 3 ? 9 : 10) * 32768, ve, aw);
      vmwait<4>();
#pragma unroll
      for (int s = 6; s < 10; ++s) wcell_read(w2[s], aw + (s - 5) * 4096, ro);
      VMWAIT0();
      wcell_read(w2[10], aw, ro);
      LGWAIT0();
    }
    STAMP(14);
    {
      int issued = 0, done = 0;
      bool fin = false;
      auto slot = [&](int i) -> u32 { return i == 2 ? a * FPART : L_AW + a * AWP + (i == 1 ? FPART : 0); };
      for (u32 s = 0; !(done == 4 && fin) && s < SPIN_G; ++s) {
        int landed = issued;
        if (issued < 4) {
          const u32 m = (u32)gflag_poll(gw, G_FLC + a * 256, tag, lane, 32);
          VMWAIT0();
          while (issued < 4 && ((m >> (8 * issued)) & 0xff) == 0xff) {
            if (issued == 2 && lflag_min4(FL_BDONE) < 3) break;
            if (issued == 3 && (done < 1 || lflag_get(FL_P4FIN) == 0)) break;
            if (issued == 0) STAMP(7);
            if (issued == 3) STAMP(15);
            const u32 dst = slot(issued);
            const char* src = gw + G_F + issued * F_TAU + a * FPART;
            dma8_sc1(src + 4096, lane * 16, dst + 4096); dma2_sc1(src + 8192, lane * 16, dst + 8192);
            if (a < 3) dma_more2048_sc1(src + 8192, lane * 16);
            ++issued;
          }
        } else VMWAIT0();
        for (;;) {
          if (!fin && done > a && (lflag_min4(FL_P4PART + a * 4) != 0)) {
            fin = true;
            const u32 st_ = a == 2 ? FPART : AWP;
            const u32 ad = (a == 2 ? 0 : L_AW + (a == 1 ? FPART : 0)) + G * 1024 + t * 16;
            v2u ov[4];
#pragma unroll
            for (int i = 0; i < 4; ++i)
              ov[i] = pk4((ldsf4(ad + i * 256) + ldsf4(ad + st_ + i * 256)) + (ldsf4(ad + 2 * st_ + i * 256) + ldsf4(ad + 3 * st_ + i * 256)));
            lflag_set(FL_P4FIN + a, 1);
            int lz = lane; asm volatile("" : "+v"(lz));
            char* op = p.out + (size_t)(T0 + 16 * a) * 1024 + j * 128 + (u32)((lz & 15) * 1024 + (lz >> 4) * 32);
            *(v4u*)op = v4u{ov[0].x, ov[0].y, ov[1].x, ov[1].y}; *(v4u*)(op + 16) = v4u{ov[2].x, ov[2].y, ov[3].x, ov[3].y};
          }
          if (done >= landed) break;
          {
            const u32 src = slot(done), sa = src + lane * 8;
            constexpr int PD = 4;
            v4u bf[11];
            SB0();
#pragma unroll
            for (int k = 0; k < PD; ++k) bf[k] = act_frag(sa + k * 1024);
            f32x4 acc[4] = {zero4, zero4, zero4, zero4};
            if (done == a) {
              asm volatile("" : "+v"(hres0), "+v"(hres1));
              acc[0] = unpk4(hres0.x, hres0.y); acc[1] = unpk4(hres0.z, hres0.w); acc[2] = unpk4(hres1.x, hres1.y); acc[3] = unpk4(hres1.z, hres1.w);
            }
            SB0();
#pragma unroll
            for (int k = 0; k < 10; ++k) {
              acc[0] = mfma32(w2[k].f[0], bf[k], acc[0]);
              SB0();
              if (k + PD < 11) bf[k + PD] = act_frag(sa + (k + PD) * 1024);
              SB0();
#pragma unroll
              for (int c = 1; c < 4; ++c) acc[c] = mfma32(w2[k].f[c], bf[k], acc[c]);
              SB0();
            }
            if (a < 3) {
#pragma unroll
              for (int c = 0; c < 4; ++c) acc[c] = mfma32(w2[10].f[c], bf[10], acc[c]);
            }
            MEMFENCE();
#pragma unroll
            for (int c = 0; c < 4; ++c) *(LAS f32x4*)(src + c * 1024 + lane * 16) = acc[c];
          }
          lflag_set(FL_P4PART + done * 4 + a, 1);
          ++done;
        }
      }
    }
    if (a == 0 && j == 0 && lane == 0) __hip_atomic_store((GAS u64*)(gw + G_EPOCH), tag, __ATOMIC_RELAXED, __HIP_MEMORY_SCOPE_AGENT);
    STAMP(8);
    STAMP_OUT();
    __builtin_amdgcn_endpgm();
  } else {
    __builtin_amdgcn_s_setprio(3);
    STAMP(7);
    const int Tj = 22 * (j >> 1) + ((j & 1) ? (j < 6 ? 12 : 10) : 0);
    const bool full = !(j & 1) && j < 6;
    const int lt = (full || b < 3) ? 3 * b : 8;
    const int nv = (full || b < 2) ? 3 : 2;
    const u32 wev = wcell_voff(lane, j);
    auto item = [&](auto I) {
      constexpr int it = decltype(I)::value;
      if constexpr (it >= 24) wcell_dma(p.wo + (size_t)(4 * b + it - 24) * 32768, wev, (it - 24) * 16384 + b * 4096);
      else if constexpr (it >= 16) xload((it - 16) / 2, it & 1);
      else if constexpr (it >= 0) wload(it);
    };
    const u32 xo0 = L_X + b * 2048 + (t >> 3) * 1024 + (t & 7) * 128 + ((G ^ ((t >> 1) & 7)) << 4);
    const int t4 = 4 * b + (t & 3);
    const u32 x4 = L_X + ((t >> 3) * 5 + 4) * 2048 + (t4 >> 3) * 1024 + (t4 & 7) * 128 + ((((t >> 2) & 1) * 4 + G) << 4);
    f32x4 s0 = zero4, q0 = zero4, s4 = zero4, q4 = zero4;
    v4u xs[2][3];
    float sq4 = 0.f;
    auto diag = [&](f32x4 v) { const float lo = (t & 1) ? v[1] : v[0], hi = (t & 1) ? v[3] : v[2]; return (t & 2) ? hi : lo; };
    auto step = [&](auto I) {
      constexpr int be = decltype(I)::value;
      vmwait<issued_before_barrier(be) - need_before_barrier(be)>();
      STAMP(12 + be);
      BARRIER();
      if constexpr (be >= 4 && be <= 10 && !NOSTATS) {
#pragma unroll
        for (int k = 0; k < 3; ++k) {
          const int i = 3 * (be - 4) + k, P = i / 5, kk = i % 5;
          if (i < 20) xs[be & 1][k] = kk < 4 ? lds128((xo0 ^ ((kk & 1) * 64)) + (2 * P + (kk >> 1)) * 10240) : lds128(x4 + P * 20480);
        }
      }
      SB0();
      item(std::integral_constant<int, SCH[be + 1][0]>{}); item(std::integral_constant<int, SCH[be + 1][1]>{});
      SB0();
      if constexpr (be >= 5 && be <= 11 && !NOSTATS) {
#pragma unroll
        for (int k = 0; k < 3; ++k) {
          constexpr int i0 = 3 * (be - 5);
          if (i0 + k < 20) {
            const v4u xx = xs[(be - 1) & 1][k];
            if ((i0 + k) % 5 < 4) { s0 = mfma32(ones, xx, s0); q0 = mfma32(xx, xx, q0); }
            else { s4 = mfma32(ones, xx, s4); q4 = mfma32(xx, xx, q4); }
          }
        }
      }
      if constexpr (be == 12 && !NOSTATS) {
        float r, bb;
        ln_coef(s0[0], diag(q0), r, bb);
        if (G == (t >> 2)) { *(LAS float*)(C_R + (16 * b + t) * 4) = r; *(LAS float*)(C_B + (16 * b + t) * 4) = bb; }
        sq4 = xg_sum(G == (t >> 2) ? diag(q4) : 0.f);
      }
      if constexpr (be == 13 && !NOSTATS) {
        float r2, b2, sm = s4[0], sq = sq4;
        sm += dppf<0x128>(sm); sq += dppf<0x128>(sq); sm += dppf<0x124>(sm); sq += dppf<0x124>(sq);
        ln_coef(sm, sq, r2, b2);
        if (lane < 4) { *(LAS float*)(C_R + (64 + t4) * 4) = r2; *(LAS float*)(C_B + (64 + t4) * 4) = b2; }
      }
    };
    [&]<int... Is>(std::integer_sequence<int, Is...>) { (step(std::integral_constant<int, Is>{}), ...); }(std::make_integer_sequence<int, 16>{});
    const int r6 = lane / 6, c6 = lane % 6;
    const u32 w1v = (16 * (r6 >> 2) + (r6 & 3)) * 2752 + min(Tj + lt + (c6 >> 1), 85) * 32 + (c6 & 1) * 16;
    const u32 aw = L_AW + b * AWP;
    auto w1load = [&](int ks) {
      if (lane < 48) dma4w(p.w1 + (size_t)(32 * ks) * 2752, w1v, w1v + 4 * 2752 - 1152, w1v + 8 * 2752 - 2048, w1v + 12 * 2752 - 3200, aw + (ks % 5) * 4096);
    };
    __builtin_amdgcn_s_setprio(0);
    BARRIER();
    STAMP(2);
    if (P1ONLY) { VMWAIT0(); STAMP_OUT(); __builtin_amdgcn_endpgm(); }
    v4u w1[16][3];
    {
#pragma unroll
      for (int ks = 0; ks < 4; ++ks) w1load(ks);
      vmwait<16>();
      lflag_set(FL_WOL + b, 1);
      const u32 ra = aw + G * 1024 + (G & 1) * 128 + ((lane & 15) >> 2) * 96 + (lane & 3) * 8;
      auto w1step = [&](auto I) {
        constexpr int ks = decltype(I)::value;
        vmwait<(ks < 13 ? 12 : (15 - ks) * 4)>();
#pragma unroll
        for (int u = 0; u < 3; ++u) { const u32 ad = ra + (ks % 5) * 4096 + u * 32; v2u lo = ldstr(ad), hi = ldstr(ad + 384); w1[ks][u] = v4u{lo.x, lo.y, hi.x, hi.y}; }
        SB0();
        if constexpr (ks + 4 < 16) { asm volatile("s_waitcnt lgkmcnt(6)" ::: "memory"); w1load(ks + 4); }
      };
      [&]<int... Is>(std::integer_sequence<int, Is...>) { (w1step(std::integral_constant<int, Is>{}), ...); }(std::make_integer_sequence<int, 16>{});
      STAMP(28);
      LGWAIT0();
#pragma unroll
      for (int k = 0; k < 5; ++k) wcell_dma(p.w2 + (size_t)(11 * b + k) * 32768, wev, aw + k * 4096);
      f32x4 cs[3] = {zero4, zero4, zero4};
#pragma unroll
      for (int ks = 0; ks < 16; ++ks)
#pragma unroll
        for (int u = 0; u < 3; ++u) cs[u] = mfma32(w1[ks][u], ones, cs[u]);
      if (t == 0) {
#pragma unroll
        for (int u = 0; u < 3; ++u) *(LAS f32x4*)(C_CS1 + (b * 3 + u) * 64 + G * 16) = cs[u];
      }
    }
    VMWAIT0();
    lflag_set(FL_W1C + b, 1);
    const v2u tg_ = lds64(C_TAG);
    const u64 tag = ((u64)uni(tg_.y) << 32) | uni(tg_.x);
    STAMP(3);
    const u32 fco = G_FLC + (j >> 1) * 256 + ((j & 1) * 4 + b) * 8;
    lwait4(FL_P2FIN, 1);
    __builtin_amdgcn_s_setprio(3);
    for (u32 s = 0; s < SPIN_G && (gflag_poll(gw, G_FLB + b * 256, tag, lane, 8) & 0xff) != 0xff; ++s) {}
    STAMP(1);
    dma1_sc1(gw + G_ST + b * 1024, lane * 16, C_S2 + b * 1024);
#pragma unroll
    for (int i = 0; i < 4; ++i) dma4_sc1(gw + G_H + (i * 16 + 4 * b) * 1024, lane * 16, i * 16384 + b * 4096);
    vmwait<12>();
    lflag_set(FL_HRDY + b, 1);
    lwait4(FL_HRDY, 1);
    STAMP(4); CSTAMP(29);
    {
      constexpr int PD = 3;
      v4u hf[64 + PD];
      f32x4 acc[4][3];
      f32x2 rb; f32x4 c4;
      f32x2 sp_[8];
      const u32 csa = C_CS1 + b * 192 + G * 16, tba = C_TB;
      const u32 hl = lane * 16;
      auto epi = [&](int i, int u) {
        f32x4 v = acc[i][u] * rb[0] + c4 * rb[1];
        const v2u fo = {pk2(fmaxf(v[0], 0.f), fmaxf(v[1], 0.f)), pk2(fmaxf(v[2], 0.f), fmaxf(v[3], 0.f))};
        if (u < 2 || nv == 3) __builtin_amdgcn_raw_buffer_store_b64(fo, rws, lane * 8, G_F + i * F_TAU + (Tj + lt + u) * 512, 16);
      };
#pragma unroll
      for (int k = 0; k < PD; ++k) hf[k] = lds128(hl + k * 1024);
      auto p3step = [&](auto I) {
        {
          constexpr int n = decltype(I)::value, i = n >> 4, ks = n & 15;
          if (ks == 0) mfma32z(acc[i][0], w1[ks][0], hf[n]); else mfma32t(acc[i][0], w1[ks][0], hf[n]);
          SB0();
          if constexpr (i == 0) {
            if constexpr (ks == 10) { VMWAIT0(); lflag_set(FL_HRDY + b, 2); }
            if constexpr (ks == 12) lwait4(FL_HRDY, 2);
            if (ks == 1) {
#pragma unroll
              for (int k = 0; k < 8; ++k) sp_[k] = ldsf2(C_S2 + (k >> 1) * 1024 + G * 256 + (k & 1) * 128 + t * 8);
            }
            if (ks == 4) { sp_[0] = ((sp_[0] + sp_[1]) + (sp_[2] + sp_[3])) + ((sp_[4] + sp_[5]) + (sp_[6] + sp_[7])); asm volatile("" : "+v"(sp_[0])); }
            if (ks == 6) { float r, bb; ln_coef(sp_[0][0], sp_[0][1], r, bb); *(LAS f32x2*)(tba + lane * 8) = f32x2{r, bb}; }
          }
          if constexpr (n + PD < 64) hf[n + PD] = lds128(hl + (n + PD) * 1024);
          if constexpr (i > 0) {
            if constexpr (ks == 1 && i > 1) gflag_pub(gw, fco + (i - 2) * 64, tag, lane);
            if (ks == 1) rb = ldsf2(tba + (i - 1) * 128 + t * 8);
            if (ks == 1 || ks == 4 || ks == 7) c4 = ldsf4(csa + (ks / 3) * 64);
            if constexpr (ks == 3 || ks == 6 || ks == 9) epi(i - 1, ks / 3 - 1);
          }
          SB0();
          if (ks == 0) { mfma32z(acc[i][1], w1[ks][1], hf[n]); mfma32z(acc[i][2], w1[ks][2], hf[n]); }
          else { mfma32t(acc[i][1], w1[ks][1], hf[n]); mfma32t(acc[i][2], w1[ks][2], hf[n]); }
          SB0();
          if constexpr (ks == 15 && i < 3) lflag_set(FL_BDONE + b, i + 1);
          if constexpr (n == 15) STAMP(8);
          if constexpr (n == 31) { STAMP(9); CSTAMP(30); }
        }
      };
      [&]<int... Is>(std::integer_sequence<int, Is...>) { (p3step(std::integral_constant<int, Is>{}), ...); }(std::make_integer_sequence<int, 64>{});
      rb = ldsf2(tba + 3 * 128 + t * 8);
      f32x4 cl[3];
#pragma unroll
      for (int u = 0; u < 3; ++u) cl[u] = ldsf4(csa + u * 64);
      gflag_pub(gw, fco + 2 * 64, tag, lane);
      asm volatile("s_nop 7" : "+v"(acc[3][0]), "+v"(acc[3][1]), "+v"(acc[3][2]));
#pragma unroll
      for (int u = 0; u < 3; ++u) { c4 = cl[u]; epi(3, u); }
    }
    STAMP(5);
    gflag_pub(gw, fco + 3 * 64, tag, lane);
    STAMP(6);
    STAMP_OUT();
    __builtin_amdgcn_endpgm();
  }
}
}

extern "C" void kernel_launch(void* const* d_in, const int* in_sizes, int n_in, void* d_out, int out_size, void* d_ws, size_t ws_size,
                              hipStream_t stream) {
  fb::Params p;
  p.x = (const char*)d_in[0]; p.wqkv = (const char*)d_in[1]; p.wo = (const char*)d_in[2];
  p.w1 = (const char*)d_in[4]; p.w2 = (const char*)d_in[5];
  p.out = (char*)d_out; p.ws = (char*)d_ws;
  (void)hipFuncSetAttribute((const void*)fb::fused_block, hipFuncAttributeMaxDynamicSharedMemorySize, fb::L_TOTAL);
  fb::fused_block<<<256, 512, fb::L_TOTAL, stream>>>(p);
}
```

```cpp
#include <hip/hip_runtime.h>
#include <hip/hip_bf16.h>
#include <utility>
#pragma clang diagnostic ignored "-Wint-to-pointer-cast"
#pragma clang diagnostic ignored "-Wc++20-extensions"

#define NOSTATS 0
#define NOMMA 0
#define P1ONLY 0
#define D1 4
#define APOLL 2
#define MAPC 1
namespace fb {
typedef unsigned u32; typedef unsigned long long u64; typedef unsigned short u16;
typedef u32 v4u __attribute__((ext_vector_type(4)));
typedef u32 v2u __attribute__((ext_vector_type(2)));
typedef float f32x4 __attribute__((ext_vector_type(4)));
typedef float f32x2 __attribute__((ext_vector_type(2)));
typedef __bf16 bf8 __attribute__((ext_vector_type(8)));
typedef __bf16 bf2 __attribute__((ext_vector_type(2)));
typedef short s4 __attribute__((ext_vector_type(4)));
#define LAS __attribute__((address_space(3)))
#define GAS __attribute__((address_space(1)))
#define DEV __device__ __forceinline__

constexpr u32 G_EPOCH = 0;
constexpr u32 FSP = 256, G_FLA = 327680, G_FLB = G_FLA + 32 * FSP, G_FLC = G_FLB + 32 * FSP;
constexpr u32 G_ST = 4096;
constexpr u32 G_O = 8192;
constexpr u32 G_H = G_O + 65536;
constexpr u32 G_F = G_H + 65536;
constexpr u32 F_TAU = 86 * 512, G_BYTES = 393216;
__host__ __device__ constexpr size_t group_base(int g) { return (size_t)(g >> 2) * (2u << 20) + (size_t)(g & 3) * G_BYTES; }
constexpr u32 WS_DBG = 16u << 20;
constexpr u32 L_X = 0, L_RING = 81920, SLOT = 12288, NSLOT = 6, L_CTRL = 155648, L_TOTAL = 163840;
constexpr u32 L_AW = 65536, AWP = 22528, FPART = 11264;
constexpr u32 C_R = L_CTRL, C_TAG = C_R + 640;
constexpr u32 C_FLAGS = L_CTRL + 1024;
constexpr u32 FL_P2PART = 0, FL_P2FIN = 16, FL_HRDY = 32, FL_WOL = 64, FL_BDONE = 68, FL_W1L = 72, FL_W1C = 76, FL_P4PART = 80, FL_P4FIN = 96, FL_HGO = 36, FL_HRD2 = 40, FL_FRDY = 44;
constexpr u32 C_CS1 = L_CTRL + 2048;
constexpr u32 C_TB = L_CTRL + 1536;
constexpr u32 C_S2 = L_CTRL + 4096;
constexpr u32 SPIN_G = 1u << 16, SPIN_L = 1u << 20;

struct Params { const char* x; const char* wqkv; const char* wo; const char* w1; const char* w2; char* out; char* ws; };

DEV f32x4 mfma32(v4u a, v4u b, f32x4 c) { return __builtin_amdgcn_mfma_f32_16x16x32_bf16(__builtin_bit_cast(bf8, a), __builtin_bit_cast(bf8, b), c, 0, 0, 0); }
DEV f32x4 mfma16(v2u a, v2u b, f32x4 c) { return __builtin_amdgcn_mfma_f32_16x16x16bf16_1k(__builtin_bit_cast(s4, a), __builtin_bit_cast(s4, b), c, 0, 0, 0); }
DEV void mfma32t(f32x4& c, v4u a, v4u b) { asm volatile("v_mfma_f32_16x16x32_bf16 %0, %1, %2, %0" : "+v"(c) : "v"(a), "v"(b)); }
DEV void mfma32z(f32x4& c, v4u a, v4u b) { asm volatile("v_mfma_f32_16x16x32_bf16 %0, %1, %2, 0" : "=&v"(c) : "v"(a), "v"(b)); }
DEV u32 pk2(float a, float b) { bf2 p = {(__bf16)a, (__bf16)b}; return __builtin_bit_cast(u32, p); }
DEV v2u pk4(f32x4 v) { return v2u{pk2(v[0], v[1]), pk2(v[2], v[3])}; }
DEV float asf(u32 u) { return __builtin_bit_cast(float, u); }
DEV u32 asu(float f) { return __builtin_bit_cast(u32, f); }
DEV f32x4 unpk4(u32 lo, u32 hi) { return f32x4{asf(lo << 16), asf(lo & 0xffff0000u), asf(hi << 16), asf(hi & 0xffff0000u)}; }
DEV u32 uni(u32 v) { return __builtin_amdgcn_readfirstlane(v); }
DEV v4u lds128(u32 a) { return *(const LAS v4u*)a; }
DEV v2u lds64(u32 a) { return *(const LAS v2u*)a; }
DEV f32x4 ldsf4(u32 a) { return *(const LAS f32x4*)a; }
DEV f32x2 ldsf2(u32 a) { return *(const LAS f32x2*)a; }
DEV float ldsf(u32 a) { return *(const LAS float*)a; }
DEV v2u ldstr(u32 a) { return __builtin_bit_cast(v2u, __builtin_amdgcn_ds_read_tr16_b64_v4i16((LAS s4*)a)); }
DEV float vmax(float a, float b) { float r; asm("v_max_f32 %0, %1, %2" : "=v"(r) : "v"(a), "v"(b)); return r; }
#define MEMFENCE() asm volatile("" ::: "memory")
#define SB0() __builtin_amdgcn_sched_barrier(0)
#define BARRIER() do { SB0(); asm volatile("s_waitcnt lgkmcnt(0)\n\ts_barrier" ::: "memory"); SB0(); } while (0)
#define SGB(m, n) __builtin_amdgcn_sched_group_barrier(m, n, 0)
#define VMWAIT0() asm volatile("s_waitcnt vmcnt(0)" ::: "memory")
#define LGWAIT0() asm volatile("s_waitcnt lgkmcnt(0)" ::: "memory")
template <int N> DEV void vmwait() { asm volatile("s_waitcnt vmcnt(%0)" ::"i"(N) : "memory"); }
#define GLD "global_load_lds_dwordx4 "
DEV void dma3(const char* sb, u32 v, u32 dst) {
  asm volatile("s_mov_b32 m0, %2\n\ts_nop 0\n\t" GLD "%0, %1\n\t" GLD "%0, %1 offset:1024\n\t" GLD "%0, %1 offset:2048" ::"v"(v), "s"(sb), "s"(dst) : "memory");
}
DEV void dma2v(const char* sb, u32 v0, u32 v1, u32 dst) {
  asm volatile("s_mov_b32 m0, %3\n\ts_nop 0\n\t" GLD "%0, %2\n\t" GLD "%1, %2 offset:1024" ::"v"(v0), "v"(v1), "s"(sb), "s"(dst) : "memory");
}
DEV void dma3v(const char* sb, u32 v0, u32 v1, u32 v2, u32 dst) {
  asm volatile("s_mov_b32 m0, %4\n\ts_nop 0\n\t" GLD "%0, %3\n\t" GLD "%1, %3 offset:1024\n\t" GLD "%2, %3 offset:2048"
               ::"v"(v0), "v"(v1), "v"(v2), "s"(sb), "s"(dst) : "memory");
}
DEV void dma4v(const char* sb, u32 v0, u32 v1, u32 v2, u32 v3, u32 dst) {
  asm volatile("s_mov_b32 m0, %5\n\ts_nop 0\n\t" GLD "%0, %4\n\t" GLD "%1, %4 offset:1024\n\t" GLD "%2, %4 offset:2048\n\t" GLD "%3, %4 offset:3072"
               ::"v"(v0), "v"(v1), "v"(v2), "v"(v3), "s"(sb), "s"(dst) : "memory");
}
DEV void dma4w(const char* sb, u32 v0, u32 v1, u32 v2, u32 v3, u32 dst) {
  asm volatile("s_mov_b32 m0, %5\n\ts_nop 0\n\t" GLD "%0, %4\n\t" GLD "%1, %4 offset:1152\n\t" GLD "%2, %4 offset:2048\n\t" GLD "%3, %4 offset:3200"
               ::"v"(v0), "v"(v1), "v"(v2), "v"(v3), "s"(sb), "s"(dst) : "memory");
}
DEV void dma1_sc1(const char* sb, u32 v, u32 dst) {
  asm volatile("s_mov_b32 m0, %2\n\ts_nop 0\n\t" GLD "%0, %1 sc1" ::"v"(v), "s"(sb), "s"(dst) : "memory");
}
DEV void dma4_sc1(const char* sb, u32 v, u32 dst) {
  asm volatile("s_mov_b32 m0, %2\n\ts_nop 0\n\t" GLD "%0, %1 sc1\n\t" GLD "%0, %1 offset:1024 sc1\n\t" GLD "%0, %1 offset:2048 sc1\n\t"
               GLD "%0, %1 offset:3072 sc1" ::"v"(v), "s"(sb), "s"(dst) : "memory");
}
DEV void dma8_sc1(const char* sb, u32 v, u32 dst) {
  asm volatile("s_mov_b32 m0, %2\n\ts_nop 0\n\t" GLD "%0, %1 offset:-4096 sc1\n\t" GLD "%0, %1 offset:-3072 sc1\n\t" GLD "%0, %1 offset:-2048 sc1\n\t"
               GLD "%0, %1 offset:-1024 sc1\n\t" GLD "%0, %1 sc1\n\t" GLD "%0, %1 offset:1024 sc1\n\t" GLD "%0, %1 offset:2048 sc1\n\t"
               GLD "%0, %1 offset:3072 sc1" ::"v"(v), "s"(sb), "s"(dst) : "memory");
}
DEV void dma2_sc1(const char* sb, u32 v, u32 dst) {
  asm volatile("s_mov_b32 m0, %2\n\ts_nop 0\n\t" GLD "%0, %1 sc1\n\t" GLD "%0, %1 offset:1024 sc1" ::"v"(v), "s"(sb), "s"(dst) : "memory");
}
DEV void dma_more2048_sc1(const char* sb, u32 v) { asm volatile(GLD "%0, %1 offset:2048 sc1" ::"v"(v), "s"(sb) : "memory"); }
DEV void lflag_set(u32 word, u32 v) {
  MEMFENCE();
  __hip_atomic_store((LAS u32*)(C_FLAGS + word * 4), v, __ATOMIC_RELAXED, __HIP_MEMORY_SCOPE_WORKGROUP);
  MEMFENCE();
}
DEV u32 lflag_min4(u32 word) {
  v4u v;
  asm volatile("ds_read_b128 %0, %1\n\ts_waitcnt lgkmcnt(0)" : "=v"(v) : "v"(C_FLAGS + word * 4) : "memory");
  u32 m = min(min(v.x, v.y), min(v.z, v.w));
  return uni(m);
}
DEV u32 lflag_get(u32 word) {
  u32 v;
  asm volatile("ds_read_b32 %0, %1\n\ts_waitcnt lgkmcnt(0)" : "=v"(v) : "v"(C_FLAGS + word * 4) : "memory");
  return uni(v);
}
DEV void lwait4(u32 word, u32 want) { for (u32 s = 0; s < SPIN_L && lflag_min4(word) < want; ++s) {} }
DEV void lwait1(u32 word, u32 want) { for (u32 s = 0; s < SPIN_L && lflag_get(word) < want; ++s) {} }
DEV void gflag_pub(char* gw, u32 off, u64 tag, int lane) {
  VMWAIT0();
  if (lane == 0) __hip_atomic_store((GAS u64*)(gw + off), tag, __ATOMIC_RELAXED, __HIP_MEMORY_SCOPE_AGENT);
  MEMFENCE();
}
DEV u64 gflag_poll(char* gw, u32 off, u64 tag, int lane, int n) {
  u64 v = __hip_atomic_load((GAS u64*)(gw + off + (lane & 7) * FSP + ((lane & (n - 1)) >> 3) * (32 * FSP)), __ATOMIC_RELAXED, __HIP_MEMORY_SCOPE_AGENT);
  u64 m = __builtin_amdgcn_ballot_w64(v == tag);
  MEMFENCE();
  return m;
}
DEV u64 mix64(u64 z) {
  z += 0x9E3779B97F4A7C15ull; z = (z ^ (z >> 30)) * 0xBF58476D1CE4E5B9ull; z = (z ^ (z >> 27)) * 0x94D049BB133111EBull; return z ^ (z >> 31);
}
DEV float xg_sum(float v) {
  auto a = __builtin_amdgcn_permlane16_swap(asu(v), asu(v), false, false);
  v = asf(a[0]) + asf(a[1]);
  auto b = __builtin_amdgcn_permlane32_swap(asu(v), asu(v), false, false);
  return asf(b[0]) + asf(b[1]);
}
template <int CTRL> DEV float dppf(float v) { return asf(__builtin_amdgcn_update_dpp(0u, asu(v), CTRL, 0xf, 0xf, false)); }
DEV void ln_coef(float s, float q, float& r, float& b) {
  float m = s * (1.0f / 512.0f);
  float var = fmaxf(q * (1.0f / 512.0f) - m * m, 0.0f);
  r = __builtin_amdgcn_rsqf(var + 1e-5f);
  b = -m * r;
}
#define STAMP(i)
#define CSTAMP(i)
#define STAMP_DECL
#define STAMP_OUT()

struct Frag4 { v4u f[4]; };
DEV void wcell_dma(const char* src, u32 ve, u32 dst) { dma4v(src, ve, (ve ^ 64) + 3072, ve + 6144, (ve ^ 64) + 9216, dst); }
DEV u32 wcell_voff(int lane, int j) {
  const int r = lane >> 3;
  return (16 * (r >> 2) + (r & 3)) * 1024 + j * 128 + ((((lane & 7) >> 1) ^ ((r >> 1) & 1)) * 32) + (lane & 1) * 16;
}
DEV u32 wcell_roff(int lane) {
  const int G = lane >> 4, q = (lane & 15) >> 2;
  return G * 1024 + q * 128 + ((((q >> 1) & 1) | ((G & 1) << 1)) * 32) + (lane & 3) * 8;
}
DEV void wcell_read(Frag4& Fg, u32 cell, u32 ro) {
#pragma unroll
  for (int c = 0; c < 4; ++c) { const u32 ad = cell + (ro ^ (c * 32)); v2u lo = ldstr(ad), hi = ldstr(ad + 512); Fg.f[c] = v4u{lo.x, lo.y, hi.x, hi.y}; }
}
DEV v4u act_frag(u32 a) { v2u lo = lds64(a), hi = lds64(a + 512); return v4u{lo.x, lo.y, hi.x, hi.y}; }

DEV void fload(const char* gw, int i, int a, u32 dst, int lane) {
  const char* src = gw + G_F + i * F_TAU + a * FPART;
  dma8_sc1(src + 4096, lane * 16, dst + 4096); dma2_sc1(src + 8192, lane * 16, dst + 8192);
  if (a < 3) dma_more2048_sc1(src + 8192, lane * 16);
}
constexpr int SCH[17][4] = {{1, 2, 3, 4},     {5, 18, -1, -1},  {6, 19, -1, -1},  {7, 20, -1, -1},  {8, 21, -1, -1},  {9, 22, -1, -1},
                            {10, 23, -1, -1}, {11, -1, -1, -1}, {12, -1, -1, -1}, {13, -1, -1, -1}, {14, -1, -1, -1}, {15, -1, -1, -1},
                            {24, -1, -1, -1}, {25, -1, -1, -1}, {26, -1, -1, -1}, {27, -1, -1, -1}, {-1, -1, -1, -1}};
constexpr int item_ops(int it) { return it < 0 ? 0 : it < 16 ? 3 : it < 24 ? ((it & 1) ? 2 : 3) : 4; }
constexpr int item_end(int it) { int n = 0; for (int gI = 0; gI < 17; ++gI) for (int k = 0; k < 4; ++k) { n += item_ops(SCH[gI][k]); if (SCH[gI][k] == it) return n; } return 0; }
constexpr int issued_before_barrier(int b) { int n = 0; for (int gI = 0; gI <= b; ++gI) for (int k = 0; k < 4; ++k) n += item_ops(SCH[gI][k]); return n; }
constexpr int xpair_at(int b) { return b < 4 ? 0 : b < 7 ? 1 : b < 9 ? 2 : 3; }
constexpr int need_before_barrier(int b) { return item_end(b) > item_end(17 + 2 * xpair_at(b)) ? item_end(b) : item_end(17 + 2 * xpair_at(b)); }

__global__ void __launch_bounds__(512, 2) fused_block(Params p) {
  extern __shared__ __attribute__((aligned(16))) char smem[];
  const int tid = threadIdx.x, lane = tid & 63;
  const int w = uni(tid >> 6);
  const int bid = blockIdx.x, xcd = bid & 7, idx = bid >> 3;
  const int g = xcd * 4 + (idx >> 3), j = idx & 7;
  const int T0 = g * 64;
  const int t = lane & 15, G = lane >> 4;
  char* const gw = p.ws + group_base(g);
  const __amdgpu_buffer_rsrc_t rws = __builtin_amdgcn_make_buffer_rsrc((void*)gw, 0, G_BYTES, 0x00020000);
  STAMP_DECL;
  STAMP(0);
  asm volatile("" ::"s"(smem));
  const int b = w & 3, rho = lane >> 3;
  const u32 wv = (8 * b + rho) * 3072 + j * 128 + (((((lane & 7) >> 1) ^ (((rho >> 1) & 1) | ((b & 1) << 1))) * 32) + (lane & 1) * 16);
  auto wload = [&](int c) { dma3(p.wqkv + (size_t)((c + 2 * xcd) & 15) * (32 * 3072), wv, L_RING + (c % NSLOT) * SLOT + b * 3072); };
  if (w < 4) wload(0); else { wload(1); wload(2); wload(3); wload(4); }
  u32 xv[5];
#pragma unroll
  for (int n = 0; n < 5; ++n) {
    const int i = (5 * b + n) % 10, tau = i >> 1, hf = i & 1;
    const int row = T0 - 16 + 16 * tau + 8 * hf + rho;
    xv[n] = max(row, 0) * 1024 + ((((lane & 7) ^ (rho >> 1)) << 4) ^ (hf * 64)) + 2048 - (n < 3 ? n : n - 3) * 1024;
  }
  auto xload = [&](int P, int part) {
    const char* src = p.x - 2048 + ((2 * P + (b >> 1) + xcd) & 7) * 128;
    if (part == 0) dma3v(src, xv[0], xv[1], xv[2], L_X + P * 20480 + 5 * b * 1024);
    else dma2v(src, xv[3], xv[4], L_X + P * 20480 + (5 * b + 3) * 1024);
  };
  if (w < 4) { xload(0, 0); xload(0, 1); }
  const v4u ones = {0x3F803F80u, 0x3F803F80u, 0x3F803F80u, 0x3F803F80u};
  const f32x4 zero4 = {0, 0, 0, 0};

  int wr_ = w;
  asm volatile("" : "+s"(wr_));
  if (wr_ < 4) {
    const int a = w;
    __builtin_amdgcn_s_setprio(2);
    u32 zz = 0; asm volatile("" : "+v"(zz));
    const u64 epv = __hip_atomic_load((GAS u64*)(gw + G_EPOCH + zz), __ATOMIC_RELAXED, __HIP_MEMORY_SCOPE_AGENT);
    if (a == 0) { *(LAS u32*)(C_FLAGS + lane * 4) = 0; *(LAS u32*)(C_FLAGS + 256 + lane * 4) = 0; }
    f32x4 aq[5], ak[5], av[5];
#pragma unroll
    for (int i = 0; i < 5; ++i) { aq[i] = zero4; ak[i] = zero4; av[i] = zero4; }
    const int q = (lane & 15) >> 2, pp = lane & 3;
    const u32 woff = L_RING + G * 3072 + q * 128 + ((a ^ (((q >> 1) & 1) | ((G & 1) << 1))) * 32) + pp * 8;
    const u32 xo0 = L_X + (t >> 3) * 1024 + (t & 7) * 128 + ((G ^ ((t >> 1) & 7)) << 4);
    struct Fr { v4u w[3]; v4u x[5]; };
    auto ldfr = [&](Fr& f, u32 wa, u32 xa) {
#pragma unroll
      for (int m = 0; m < 3; ++m) { v2u lo = ldstr(wa + m * 1024), hi = ldstr(wa + m * 1024 + 512); f.w[m] = v4u{lo.x, lo.y, hi.x, hi.y}; }
#pragma unroll
      for (int i = 0; i < 5; ++i) f.x[i] = lds128(xa + i * 2048);
    };
    auto mma = [&](Fr& f) {
      if (NOMMA) { for (int i = 0; i < 5; ++i) asm volatile("" :: "v"(f.x[i])); for (int i = 0; i < 3; ++i) asm volatile("" :: "v"(f.w[i])); return; }
      v4u x0 = f.x[0];
      if (t == 0) x0 = ones;
#pragma unroll
      for (int i = 1; i < 5; ++i) { aq[i] = mfma32(f.w[0], f.x[i], aq[i]); ak[i] = mfma32(f.w[1], f.x[i], ak[i]); av[i] = mfma32(f.x[i], f.w[2], av[i]); }
      aq[0] = mfma32(f.w[0], x0, aq[0]); ak[0] = mfma32(f.w[1], x0, ak[0]); av[0] = mfma32(x0, f.w[2], av[0]);
    };
    Fr f0, f1;
    u32 so = 0, xa = xo0;
#pragma unroll
    for (int i = 0; i < 5; ++i) asm volatile("" : "+v"(aq[i]), "+v"(ak[i]), "+v"(av[i]));
    VMWAIT0();
    BARRIER();
    STAMP(1); CSTAMP(24);
    ldfr(f0, woff, xa);
    SB0();
    const u64 tag = mix64(((u64)uni((u32)(epv >> 32)) << 32) | uni((u32)epv));
    if (a == 0 && lane == 0) *(LAS u64*)C_TAG = tag;
    v2u xres[4];
    const u32 xra = L_X + (((j - xcd) & 7) * 5 + a + 1) * 2048 + (t >> 3) * 1024 + (t & 7) * 128 + (((G >> 1) ^ ((t >> 1) & 7)) << 4) + (G & 1) * 8;
    auto pat = [&]() {
#pragma unroll
      for (int i = 0; i < 6; ++i) { SGB(0x8, 1); SGB(0x100, 2); }
      SGB(0x8, 9);
    };
#pragma unroll 1
    for (int it = 0; it < 7; ++it) {
      so = (so == (NSLOT - 1) * SLOT) ? 0 : so + SLOT;
      BARRIER();
      ldfr(f1, woff + so, xa ^ 64);
      mma(f0);
      pat();
      so = (so == (NSLOT - 1) * SLOT) ? 0 : so + SLOT;
      xa += 10240;
      BARRIER();
      ldfr(f0, woff + so, xa);
      mma(f1);
      pat();
      SB0();
      if (it <= 4) {
#pragma unroll
        for (int c = 0; c < 4; ++c) xres[c] = lds64(xra ^ (c * 32));
      }
    }
    BARRIER();
    ldfr(f1, woff + 3 * SLOT, xa ^ 64);
    f32x2 rb1[5]; f32x4 rbA[5], rbB[5];
    {
      u32 ca = C_R + t * 8, cb = C_R + G * 32;
      asm volatile("" : "+v"(ca), "+v"(cb));
#pragma unroll
      for (int i = 0; i < 5; ++i) { rb1[i] = ldsf2(ca + i * 128); rbA[i] = ldsf4(cb + i * 128); rbB[i] = ldsf4(cb + i * 128 + 16); }
    }
    mma(f0);
#pragma unroll
    for (int i = 0; i < 13; ++i) { SGB(0x8, 1); SGB(0x100, 2); }
    SGB(0x8, 2);
    BARRIER();
    {
      v4u x0 = f1.x[0];
      if (t == 0) x0 = ones;
      aq[0] = mfma32(f1.w[0], x0, aq[0]); ak[0] = mfma32(f1.w[1], x0, ak[0]); av[0] = mfma32(x0, f1.w[2], av[0]);
#pragma unroll
      for (int i = 1; i < 5; ++i) { ak[i] = mfma32(f1.w[1], f1.x[i], ak[i]); aq[i] = mfma32(f1.w[0], f1.x[i], aq[i]); }
#pragma unroll
      for (int i = 1; i < 5; ++i) av[i] = mfma32(f1.x[i], f1.w[2], av[i]);
    }
    STAMP(2); CSTAMP(25);
    {
      v2u kb[5], vb[5], qb[5];
      f32x4 csq, csk;
#pragma unroll
      for (int r = 0; r < 4; ++r) { csq[r] = dppf<0x150>(aq[0][r]); csk[r] = dppf<0x150>(ak[0][r]); }
      const float csv = __shfl(av[0][0], t);
#pragma unroll
      for (int i = 0; i < 5; ++i) {
        kb[i] = pk4(ak[i] * rb1[i][0] + csk * rb1[i][1]);
        if (i > 0) qb[i] = pk4(aq[i] * rb1[i][0] + csq * rb1[i][1]);
      }
      SB0();
      f32x4 sp[4], sc[4];
#pragma unroll
      for (int i = 0; i < 4; ++i) { sp[i] = mfma16(kb[i], qb[i + 1], zero4); sc[i] = mfma16(kb[i + 1], qb[i + 1], zero4); }
#pragma unroll
      for (int i = 0; i < 5; ++i)
        vb[i] = v2u{pk2(av[i][0] * rbA[i][0] + csv * rbA[i][1], av[i][1] * rbA[i][2] + csv * rbA[i][3]),
                    pk2(av[i][2] * rbB[i][0] + csv * rbB[i][1], av[i][3] * rbB[i][2] + csv * rbB[i][3])};
      SB0();
      const bool par = t & 1;
      const bool cur0 = 4 * G + (t & 1) <= t, cur1 = 4 * G + (t & 1) + 2 <= t;
      const u32 sh = (t & 1) * 16;
      float z0[4], z1[4], m[4];
#pragma unroll
      for (int i = 0; i < 4; ++i) {
        const float c0 = par ? sc[i][1] : sc[i][0], c1 = par ? sc[i][3] : sc[i][2];
        float p0 = par ? sp[i][1] : sp[i][0], p1 = par ? sp[i][3] : sp[i][2];
        if (i == 0 && g == 0) { p0 = -4e9f; p1 = -4e9f; }
        z0[i] = cur0 ? c0 : p0; z1[i] = cur1 ? c1 : p1;
        m[i] = vmax(z0[i], z1[i]);
      }
      SB0();
#pragma unroll
      for (int i = 0; i < 4; ++i) { auto s_ = __builtin_amdgcn_permlane16_swap(asu(m[i]), asu(m[i]), false, false); m[i] = vmax(asf(s_[0]), asf(s_[1])); }
      SB0();
#pragma unroll
      for (int i = 0; i < 4; ++i) { auto s_ = __builtin_amdgcn_permlane32_swap(asu(m[i]), asu(m[i]), false, false); m[i] = vmax(asf(s_[0]), asf(s_[1])); }
      SB0();
      f32x4 o[4], den[4];
#pragma unroll
      for (int i = 0; i < 4; ++i) {
        const float e0 = __builtin_amdgcn_exp2f((z0[i] - m[i]) * 0.36067376022f), e1 = __builtin_amdgcn_exp2f((z1[i] - m[i]) * 0.36067376022f);
        const u32 u = pk2(e0, e1);
        const u32 w0 = (u & 0xffffu) << sh, w1 = (u >> 16) << sh;
        const v4u P = {cur0 ? 0u : w0, cur1 ? 0u : w1, cur0 ? w0 : 0u, cur1 ? w1 : 0u};
        const v4u V = {vb[i].x, vb[i].y, vb[i + 1].x, vb[i + 1].y};
        den[i] = mfma32(ones, P, zero4);
        o[i] = mfma32(V, P, zero4);
      }
      SB0();
#pragma unroll
      for (int i = 0; i < 4; ++i) {
        const v2u ob = pk4(o[i] * __builtin_amdgcn_rcpf(den[i][0]));
        __builtin_amdgcn_raw_buffer_store_b64(ob, rws, lane * 8, G_O + (i * 32 + 4 * j + a) * 512, 16);
      }
    }
    STAMP(23);
    if (P1ONLY) { STAMP(8); STAMP_OUT(); __builtin_amdgcn_endpgm(); }
    const u32 ro = wcell_roff(lane);
    Frag4 w2[11];
    v4u hres0, hres1;
    {
      Frag4 wo[4];
      lwait1(FL_WOL + a, 1);
#pragma unroll
      for (int s = 0; s < 4; ++s) wcell_read(wo[s], s * 16384 + a * 4096, ro);
      gflag_pub(gw, G_FLA + (j * 4 + a) * FSP, tag, lane);
      STAMP(3);
      LGWAIT0();
      lflag_set(FL_P2PART + a * 4 + a, 1);
      __builtin_amdgcn_s_setprio(APOLL);
      for (u32 s = 0; s < SPIN_G && (gflag_poll(gw, G_FLA + a * 8 * FSP, tag, lane, 8) & 0xff) != 0xff; ++s) {}
      __builtin_amdgcn_s_setprio(2);
      STAMP(4);
      u32 cell[4];
#pragma unroll
      for (int i = 0; i < 4; ++i) {
        const int tl = (a + 1 + i) & 3;
        cell[i] = tl * 16384 + a * 4096;
        dma4_sc1(gw + G_O + (tl * 32 + 8 * a) * 512, lane * 16, cell[i]);
      }
      v4u bf[4][4];
      f32x4 acc[4][4];
      auto rd = [&](int i) {
#pragma unroll
        for (int s = 0; s < 4; ++s) bf[i][s] = act_frag(cell[i] + lane * 8 + s * 1024);
      };
      auto mm = [&](int i) {
#pragma unroll
        for (int s = 0; s < 4; ++s)
#pragma unroll
          for (int c = 0; c < 4; ++c) acc[i][c] = mfma32(wo[s].f[c], bf[i][s], (s == 0 && i < 3) ? zero4 : acc[i][c]);
      };
      auto wr = [&](int i) {
#pragma unroll
        for (int c = 0; c < 4; ++c) *(LAS f32x4*)(cell[i] + c * 1024 + lane * 16) = acc[i][c];
        lflag_set(FL_P2PART + ((a + 1 + i) & 3) * 4 + a, 1);
      };
#pragma unroll
      for (int c = 0; c < 4; ++c) acc[3][c] = unpk4(xres[c].x, xres[c].y);
      vmwait<12>(); rd(0);
      STAMP(9);
      vmwait<8>(); rd(1); SB0();
      mm(0); SB0();
      vmwait<4>(); rd(2); SB0();
      mm(1); wr(0); SB0();
      vmwait<0>(); rd(3); SB0();
      mm(2); wr(1); SB0();
      wr(2); SB0();
      mm(3); SB0();
      STAMP(16);
      lwait4(FL_P2PART + a * 4, 1);
      STAMP(17);
      {
        const u32 pb = a * 16384 + lane * 16;
        const u32 o1 = ((a + 1) & 3) * 4096, o2 = ((a + 2) & 3) * 4096, o3 = ((a + 3) & 3) * 4096;
        f32x4 pv[4];
#pragma unroll
        for (int c = 0; c < 4; ++c) pv[c] = (ldsf4(pb + o1 + c * 1024) + ldsf4(pb + o2 + c * 1024)) + ldsf4(pb + o3 + c * 1024);
#pragma unroll
        for (int c = 0; c < 4; ++c) acc[3][c] += pv[c];
      }
      const v2u h0 = pk4(acc[3][0]), h1 = pk4(acc[3][1]), h2 = pk4(acc[3][2]), h3 = pk4(acc[3][3]);
      hres0 = v4u{h0.x, h0.y, h1.x, h1.y}; hres1 = v4u{h2.x, h2.y, h3.x, h3.y};
      const u32 ho = G_H + (a * 16 + 2 * j) * 1024;
      __builtin_amdgcn_raw_buffer_store_b128(hres0, rws, lane * 16, ho, 16);
      __builtin_amdgcn_raw_buffer_store_b128(hres1, rws, lane * 16 + 1024, ho, 16);
      SB0();
      lflag_set(FL_P2FIN + a, 1);
      {
        const f32x4 s4 = (acc[3][0] + acc[3][1]) + (acc[3][2] + acc[3][3]);
        const f32x4 q4 = (acc[3][0] * acc[3][0] + acc[3][1] * acc[3][1]) + (acc[3][2] * acc[3][2] + acc[3][3] * acc[3][3]);
        const float sm = xg_sum((s4[0] + s4[1]) + (s4[2] + s4[3])), sq = xg_sum((q4[0] + q4[1]) + (q4[2] + q4[3]));
        if (lane < 16) __builtin_amdgcn_raw_buffer_store_b64(v2u{asu(sm), asu(sq)}, rws, lane * 8, G_ST + (j >> 1) * 1024 + (a * 2 + (j & 1)) * 128, 16);
      }
      STAMP(12);
      gflag_pub(gw, G_FLB + (j * 4 + a) * FSP, tag, lane);
    }
    STAMP(5);
    __builtin_amdgcn_s_setprio(0);
    {
      const u32 aw = L_AW + a * AWP;
      const u32 ve = wcell_voff(lane, j);
      const char* w2s = p.w2 + (size_t)(11 * a) * 32768;
      lwait1(FL_W1C + a, 1);
#pragma unroll
      for (int s = 0; s < 5; ++s) wcell_read(w2[s], aw + s * 4096, ro);
      STAMP(13);
      lwait1(FL_HGO + a, 1);
#pragma unroll
      for (int i = 1; i < 4; ++i) dma4_sc1(gw + G_H + (i * 16 + 4 * a) * 1024, lane * 16, i * 16384 + a * 4096);
      VMWAIT0();
      lflag_set(FL_HRD2 + a, 1);
      STAMP(6);
#pragma unroll
      for (int s = 5; s < 10; ++s) wcell_dma(w2s + s * 32768, ve, aw + (s - 5) * 4096);
      vmwait<16>();
      wcell_read(w2[5], aw, ro);
      LGWAIT0();
      wcell_dma(w2s + (a == 3 ? 9 : 10) * 32768, ve, aw);
      vmwait<4>();
#pragma unroll
      for (int s = 6; s < 10; ++s) wcell_read(w2[s], aw + (s - 5) * 4096, ro);
      VMWAIT0();
      wcell_read(w2[10], aw, ro);
      LGWAIT0();
    }
    STAMP(14);
    {
      int issued = 0, done = 0;
      bool fin = false;
      auto slot = [&](int i) -> u32 { return i == 2 ? a * FPART : L_AW + a * AWP + (i == 1 ? FPART : 0); };
      for (u32 s = 0; !(done == 4 && fin) && s < SPIN_L; ++s) {
        int landed = issued;
        if (issued < 2) {
          const u32 m = (u32)gflag_poll(gw, G_FLC + a * 8 * FSP, tag, lane, 16);
          VMWAIT0();
          while (issued < 2 && ((m >> (8 * issued)) & 0xff) == 0xff) {
            if (issued == 0) STAMP(7);
            if (issued == 1) STAMP(18);
            fload(gw, issued, a, slot(issued), lane);
            ++issued;
          }
          s += 15;
        } else if (done < 2) VMWAIT0();
        else landed = max(2u, lflag_get(FL_FRDY + a));
        for (;;) {
          if (!fin && done > a && (lflag_min4(FL_P4PART + a * 4) != 0)) {
            fin = true;
            const u32 st_ = a == 2 ? FPART : AWP;
            const u32 ad = (a == 2 ? 0 : L_AW + (a == 1 ? FPART : 0)) + G * 1024 + t * 16;
            v2u ov[4];
#pragma unroll
            for (int i = 0; i < 4; ++i)
              ov[i] = pk4((ldsf4(ad + i * 256) + ldsf4(ad + st_ + i * 256)) + (ldsf4(ad + 2 * st_ + i * 256) + ldsf4(ad + 3 * st_ + i * 256)));
            lflag_set(FL_P4FIN + a, 1);
            int lz = lane; asm volatile("" : "+v"(lz));
            char* op = p.out + (size_t)(T0 + 16 * a) * 1024 + j * 128 + (u32)((lz & 15) * 1024 + (lz >> 4) * 32);
            *(v4u*)op = v4u{ov[0].x, ov[0].y, ov[1].x, ov[1].y}; *(v4u*)(op + 16) = v4u{ov[2].x, ov[2].y, ov[3].x, ov[3].y};
          }
          if (done >= landed) break;
          {
            const u32 src = slot(done), sa = src + lane * 8;
            constexpr int PD = 4;
            v4u bf[11];
            SB0();
#pragma unroll
            for (int k = 0; k < PD; ++k) bf[k] = act_frag(sa + k * 1024);
            f32x4 acc[4] = {zero4, zero4, zero4, zero4};
            if (done == a) {
              asm volatile("" : "+v"(hres0), "+v"(hres1));
              acc[0] = unpk4(hres0.x, hres0.y); acc[1] = unpk4(hres0.z, hres0.w); acc[2] = unpk4(hres1.x, hres1.y); acc[3] = unpk4(hres1.z, hres1.w);
            }
            SB0();
#pragma unroll
            for (int k = 0; k < 10; ++k) {
              acc[0] = mfma32(w2[k].f[0], bf[k], acc[0]);
              SB0();
              if (k + PD < 11) bf[k + PD] = act_frag(sa + (k + PD) * 1024);
              SB0();
#pragma unroll
              for (int c = 1; c < 4; ++c) acc[c] = mfma32(w2[k].f[c], bf[k], acc[c]);
              SB0();
            }
            if (a < 3) {
#pragma unroll
              for (int c = 0; c < 4; ++c) acc[c] = mfma32(w2[10].f[c], bf[10], acc[c]);
            }
            MEMFENCE();
#pragma unroll
            for (int c = 0; c < 4; ++c) *(LAS f32x4*)(src + c * 1024 + lane * 16) = acc[c];
          }
          lflag_set(FL_P4PART + done * 4 + a, 1);
          if (done == 0) STAMP(20); if (done == 1) STAMP(21); if (done == 2) STAMP(22); if (done == 3) STAMP(26);
          ++done;
        }
      }
    }
    if (a == 0 && j == 0 && lane == 0) __hip_atomic_store((GAS u64*)(gw + G_EPOCH), tag, __ATOMIC_RELAXED, __HIP_MEMORY_SCOPE_AGENT);
    STAMP(8);
    STAMP_OUT();
    __builtin_amdgcn_endpgm();
  } else {
    __builtin_amdgcn_s_setprio(3);
    STAMP(7);
    const int Tj = 22 * (j >> 1) + ((j & 1) ? (j < 6 ? 12 : 10) : 0);
    const bool full = !(j & 1) && j < 6;
    const int lt = (full || b < 3) ? 3 * b : 8;
    const int nv = (full || b < 2) ? 3 : 2;
    const u32 wev = wcell_voff(lane, j);
    auto item = [&](auto I) {
      constexpr int it = decltype(I)::value;
      if constexpr (it >= 24) wcell_dma(p.wo + (size_t)(4 * b + it - 24) * 32768, wev, (it - 24) * 16384 + b * 4096);
      else if constexpr (it >= 16) xload((it - 16) / 2, it & 1);
      else if constexpr (it >= 0) wload(it);
    };
    const u32 xo0 = L_X + b * 2048 + (t >> 3) * 1024 + (t & 7) * 128 + ((G ^ ((t >> 1) & 7)) << 4);
    const int t4 = 4 * b + (t & 3);
    const u32 x4 = L_X + ((t >> 3) * 5 + 4) * 2048 + (t4 >> 3) * 1024 + (t4 & 7) * 128 + ((((t >> 2) & 1) * 4 + G) << 4);
    f32x4 s0 = zero4, q0 = zero4, s4 = zero4, q4 = zero4;
    v4u xs[2][3];
    float sq4 = 0.f;
    auto diag = [&](f32x4 v) { const float lo = (t & 1) ? v[1] : v[0], hi = (t & 1) ? v[3] : v[2]; return (t & 2) ? hi : lo; };
    auto step = [&](auto I) {
      constexpr int be = decltype(I)::value;
      vmwait<issued_before_barrier(be) - need_before_barrier(be)>();
      BARRIER();
      if constexpr (be >= 4 && be <= 10 && !NOSTATS) {
#pragma unroll
        for (int k = 0; k < 3; ++k) {
          const int i = 3 * (be - 4) + k, P = i / 5, kk = i % 5;
          if (i < 20) xs[be & 1][k] = kk < 4 ? lds128((xo0 ^ ((kk & 1) * 64)) + (2 * P + (kk >> 1)) * 10240) : lds128(x4 + P * 20480);
        }
      }
      SB0();
      item(std::integral_constant<int, SCH[be + 1][0]>{}); item(std::integral_constant<int, SCH[be + 1][1]>{});
      SB0();
      if constexpr (be >= 5 && be <= 11 && !NOSTATS) {
#pragma unroll
        for (int k = 0; k < 3; ++k) {
          constexpr int i0 = 3 * (be - 5);
          if (i0 + k < 20) {
            const v4u xx = xs[(be - 1) & 1][k];
            if ((i0 + k) % 5 < 4) { s0 = mfma32(ones, xx, s0); q0 = mfma32(xx, xx, q0); }
            else { s4 = mfma32(ones, xx, s4); q4 = mfma32(xx, xx, q4); }
          }
        }
      }
      if constexpr (be == 12 && !NOSTATS) {
        float r, bb;
        ln_coef(s0[0], diag(q0), r, bb);
        if (G == (t >> 2)) *(LAS f32x2*)(C_R + (16 * b + t) * 8) = f32x2{r, bb};
        sq4 = xg_sum(G == (t >> 2) ? diag(q4) : 0.f);
      }
      if constexpr (be == 13 && !NOSTATS) {
        float r2, b2, sm = s4[0], sq = sq4;
        sm += dppf<0x128>(sm); sq += dppf<0x128>(sq); sm += dppf<0x124>(sm); sq += dppf<0x124>(sq);
        ln_coef(sm, sq, r2, b2);
        if (lane < 4) *(LAS f32x2*)(C_R + (64 + t4) * 8) = f32x2{r2, b2};
      }
    };
    [&]<int... Is>(std::integer_sequence<int, Is...>) { (step(std::integral_constant<int, Is>{}), ...); }(std::make_integer_sequence<int, 16>{});
    const int r6 = lane / 6, c6 = lane % 6;
    const u32 w1v = (16 * (r6 >> 2) + (r6 & 3)) * 2752 + min(Tj + lt + (c6 >> 1), 85) * 32 + (c6 & 1) * 16;
    const u32 aw = L_AW + b * AWP;
    auto w1load = [&](int ks) {
      if (lane < 48) dma4w(p.w1 + (size_t)(32 * ks) * 2752, w1v, w1v + 4 * 2752 - 1152, w1v + 8 * 2752 - 2048, w1v + 12 * 2752 - 3200, aw + (ks % 5) * 4096);
    };
    __builtin_amdgcn_s_setprio(1);
    BARRIER();
    STAMP(2);
    if (P1ONLY) { VMWAIT0(); STAMP_OUT(); __builtin_amdgcn_endpgm(); }
    v4u w1[16][3];
    {
#pragma unroll
      for (int ks = 0; ks < D1; ++ks) w1load(ks);
      vmwait<4 * D1>();
      lflag_set(FL_WOL + b, 1);
      const u32 ra = aw + G * 1024 + (G & 1) * 128 + ((lane & 15) >> 2) * 96 + (lane & 3) * 8;
      auto w1step = [&](auto I) {
        constexpr int ks = decltype(I)::value;
        vmwait<(ks + D1 <= 16 ? 4 * (D1 - 1) : (15 - ks) * 4)>();
#pragma unroll
        for (int u = 0; u < 3; ++u) { const u32 ad = ra + (ks % 5) * 4096 + u * 32; v2u lo = ldstr(ad), hi = ldstr(ad + 384); w1[ks][u] = v4u{lo.x, lo.y, hi.x, hi.y}; }
        SB0();
        if constexpr (ks + D1 < 16) { asm volatile("s_waitcnt lgkmcnt(6)" ::: "memory"); w1load(ks + D1); }
      };
      [&]<int... Is>(std::integer_sequence<int, Is...>) { (w1step(std::integral_constant<int, Is>{}), ...); }(std::make_integer_sequence<int, 16>{});
      STAMP(28);
      LGWAIT0();
#pragma unroll
      for (int k = 0; k < 5; ++k) wcell_dma(p.w2 + (size_t)(11 * b + k) * 32768, wev, aw + k * 4096);
      f32x4 cs[3] = {zero4, zero4, zero4};
#pragma unroll
      for (int ks = 0; ks < 16; ++ks)
#pragma unroll
        for (int u = 0; u < 3; ++u) cs[u] = mfma32(w1[ks][u], ones, cs[u]);
      if (t == 0) {
#pragma unroll
        for (int u = 0; u < 3; ++u) *(LAS f32x4*)(C_CS1 + (b * 3 + u) * 64 + G * 16) = cs[u];
      }
    }
    VMWAIT0();
    lflag_set(FL_W1C + b, 1);
    const v2u tg_ = lds64(C_TAG);
    const u64 tag = ((u64)uni(tg_.y) << 32) | uni(tg_.x);
    STAMP(3);
    const u32 fco = G_FLC + (j * 4 + b) * FSP;
    lwait4(FL_P2FIN, 1);
    __builtin_amdgcn_s_setprio(3);
    for (u32 s = 0; s < SPIN_G && (gflag_poll(gw, G_FLB + b * 8 * FSP, tag, lane, 8) & 0xff) != 0xff; ++s) {}
    STAMP(1);
    dma1_sc1(gw + G_ST + b * 1024, lane * 16, C_S2 + b * 1024);
    dma4_sc1(gw + G_H + (4 * b) * 1024, lane * 16, b * 4096);
    lflag_set(FL_HGO + b, 1);
    VMWAIT0();
    lflag_set(FL_HRDY + b, 1);
    lwait4(FL_HRDY, 1);
    STAMP(4); CSTAMP(29);
    {
      constexpr int PD = 3;
      v4u hf[64 + PD];
      f32x4 acc[4][3];
      f32x2 rb; f32x4 c4;
      f32x2 sp_[8];
      const u32 csa = C_CS1 + b * 192 + G * 16, tba = C_TB;
      const u32 hl = lane * 16;
      auto epi = [&](int i, int u) {
        f32x4 v = acc[i][u] * rb[0] + c4 * rb[1];
        const v2u fo = {pk2(fmaxf(v[0], 0.f), fmaxf(v[1], 0.f)), pk2(fmaxf(v[2], 0.f), fmaxf(v[3], 0.f))};
        if (u < 2 || nv == 3) __builtin_amdgcn_raw_buffer_store_b64(fo, rws, lane * 8, G_F + i * F_TAU + (Tj + lt + u) * 512, 16);
      };
#pragma unroll
      for (int k = 0; k < PD; ++k) hf[k] = lds128(hl + k * 1024);
      auto p3step = [&](auto I) {
        {
          constexpr int n = decltype(I)::value, i = n >> 4, ks = n & 15;
          if (ks == 0) mfma32z(acc[i][0], w1[ks][0], hf[n]); else mfma32t(acc[i][0], w1[ks][0], hf[n]);
          SB0();
          if constexpr (i == 0) {
            if constexpr (ks == 12) lwait4(FL_HRD2, 1);
            if (ks == 1) {
#pragma unroll
              for (int k = 0; k < 8; ++k) sp_[k] = ldsf2(C_S2 + (k >> 1) * 1024 + G * 256 + (k & 1) * 128 + t * 8);
            }
            if (ks == 4) { sp_[0] = ((sp_[0] + sp_[1]) + (sp_[2] + sp_[3])) + ((sp_[4] + sp_[5]) + (sp_[6] + sp_[7])); asm volatile("" : "+v"(sp_[0])); }
            if (ks == 6) { float r, bb; ln_coef(sp_[0][0], sp_[0][1], r, bb); *(LAS f32x2*)(tba + lane * 8) = f32x2{r, bb}; }
          }
          if constexpr (n + PD < 64) hf[n + PD] = lds128(hl + (n + PD) * 1024);
          if constexpr (i > 0) {
            if constexpr (ks == 1 && i > 1) { STAMP(12 + 2 * (i - 2)); gflag_pub(gw, fco + (i - 2) * 32 * FSP, tag, lane); STAMP(13 + 2 * (i - 2)); }
            if (ks == 1) rb = ldsf2(tba + (i - 1) * 128 + t * 8);
            if (ks == 1 || ks == 4 || ks == 7) c4 = ldsf4(csa + (ks / 3) * 64);
            if constexpr (ks == 3 || ks == 6 || ks == 9) epi(i - 1, ks / 3 - 1);
          }
          SB0();
          if (ks == 0) { mfma32z(acc[i][1], w1[ks][1], hf[n]); mfma32z(acc[i][2], w1[ks][2], hf[n]); }
          else { mfma32t(acc[i][1], w1[ks][1], hf[n]); mfma32t(acc[i][2], w1[ks][2], hf[n]); }
          SB0();
          if constexpr (ks == 15 && i < 3) lflag_set(FL_BDONE + b, i + 1);
          if constexpr (n == 15) STAMP(8);
          if constexpr (n == 31) { STAMP(9); CSTAMP(30); }
        }
      };
      [&]<int... Is>(std::integer_sequence<int, Is...>) { (p3step(std::integral_constant<int, Is>{}), ...); }(std::make_integer_sequence<int, 64>{});
      rb = ldsf2(tba + 3 * 128 + t * 8);
      f32x4 cl[3];
#pragma unroll
      for (int u = 0; u < 3; ++u) cl[u] = ldsf4(csa + u * 64);
      STAMP(16);
      gflag_pub(gw, fco + 2 * 32 * FSP, tag, lane);
      STAMP(17);
      asm volatile("s_nop 7" : "+v"(acc[3][0]), "+v"(acc[3][1]), "+v"(acc[3][2]));
#pragma unroll
      for (int u = 0; u < 3; ++u) { c4 = cl[u]; epi(3, u); }
    }
    STAMP(5);
    {
      int issued = 2;
      bool pub = false;
      for (u32 s = 0; issued < 4 && s < SPIN_G; ++s) {
        const u32 m = (u32)gflag_poll(gw, G_FLC + (64 + b * 8) * FSP, tag, lane, 16) << 16;
        if (!pub) { gflag_pub(gw, fco + 3 * 32 * FSP, tag, lane); pub = true; STAMP(6); }
        else { VMWAIT0(); lflag_set(FL_FRDY + b, issued); }
        while (issued < 4 && ((m >> (8 * issued)) & 0xff) == 0xff) {
          if (issued == 2) lwait4(FL_BDONE, 3);
          if (issued == 3 && lflag_get(FL_P4FIN) == 0) break;
          if (issued == 2) STAMP(19);
          if (issued == 3) STAMP(20);
          fload(gw, issued, b, issued == 2 ? b * FPART : L_AW + b * AWP, lane);
          ++issued;
          if (issued == 4) { vmwait<10>(); lflag_set(FL_FRDY + b, 3); }
        }
      }
      VMWAIT0();
      lflag_set(FL_FRDY + b, 4);
      STAMP(21);
    }
    STAMP_OUT();
    __builtin_amdgcn_endpgm();
  }
}
}

extern "C" void kernel_launch(void* const* d_in, const int* in_sizes, int n_in, void* d_out, int out_size, void* d_ws, size_t ws_size,
                              hipStream_t stream) {
  fb::Params p;
  p.x = (const char*)d_in[0]; p.wqkv = (const char*)d_in[1]; p.wo = (const char*)d_in[2];
  p.w1 = (const char*)d_in[4]; p.w2 = (const char*)d_in[5];
  p.out = (char*)d_out; p.ws = (char*)d_ws;
  (void)hipFuncSetAttribute((const void*)fb::fused_block, hipFuncAttributeMaxDynamicSharedMemorySize, fb::L_TOTAL);
  fb::fused_block<<<256, 512, fb::L_TOTAL, stream>>>(p);
}
```

```cpp
#include <hip/hip_runtime.h>
#include <hip/hip_bf16.h>
#include <utility>
#pragma clang diagnostic ignored "-Wint-to-pointer-cast"
#pragma clang diagnostic ignored "-Wc++20-extensions"
#define MAPC 1
#define TOUCH 3
#define TW 2
#define TSTEP 65536u
#define TO 5
#define FLW 1
#define P1ONLY 0
namespace fb {
typedef unsigned u32; typedef unsigned long long u64; typedef unsigned short u16;
typedef u32 v4u __attribute__((ext_vector_type(4)));
typedef u32 v2u __attribute__((ext_vector_type(2)));
typedef float f32x4 __attribute__((ext_vector_type(4)));
typedef float f32x2 __attribute__((ext_vector_type(2)));
typedef __bf16 bf8 __attribute__((ext_vector_type(8)));
typedef __bf16 bf2 __attribute__((ext_vector_type(2)));
typedef short s4 __attribute__((ext_vector_type(4)));
#define LAS __attribute__((address_space(3)))
#define GAS __attribute__((address_space(1)))
#define DEV __device__ __forceinline__
#define IC(x) std::integral_constant<int, (x)>{}
#define UNROLL_SEQ(fn, N) [&]<int... Is_>(std::integer_sequence<int, Is_...>) { (fn(std::integral_constant<int, Is_>{}), ...); }(std::make_integer_sequence<int, N>{})

constexpr u32 G_EPOCH = 0;
constexpr u32 G_O = 8192;
constexpr u32 G_H = G_O + 65536;
constexpr u32 G_F = G_H + 65536;
constexpr u32 F_TAU = 86 * 512;
constexpr u32 G_ST = 315392 + 1024;
constexpr u32 FSP = 256, FBL = 32 * FSP;
constexpr u32 G_FLA = 327680, G_FLB = G_FLA + 2 * FBL, G_FLC = G_FLB + 2 * FBL, G_BYTES = G_FLC + 4 * FBL;
static_assert(4 * G_BYTES <= (2u << 20) && G_ST + 8192 <= G_FLA, "workspace map");
__host__ __device__ constexpr size_t group_base(int g) { return (size_t)(g >> 2) * (2u << 20) + (size_t)(g & 3) * G_BYTES; }
constexpr u32 WS_DBG = 16u << 20;
constexpr u32 L_X = 0, L_RING = 81920, SLOT = 12288, L_CTRL = 155648, L_TOTAL = 163840;
constexpr u32 L_W2 = 65536, L_P3 = 114688, C_S2 = 147456;
constexpr u32 FPART = 11264, L_F1 = 65536, L_F2 = 110592, L_P4 = 45056, PQ = 4352;
constexpr u32 C_R = L_CTRL;
constexpr u32 C_CS1 = L_CTRL + 1024;
constexpr u32 C_FLAGS = L_CTRL + 768, FL_W1L = 0, FL_W1C = 4, C_TAG = L_CTRL + 640, NSLOT = 6;
constexpr u32 SPIN_G = 1u << 16, SPIN_L = 1u << 20;

struct Params { const char* x; const char* wqkv; const char* wo; const char* w1; const char* w2; char* out; char* ws; };

DEV f32x4 mfma32(v4u a, v4u b, f32x4 c) { return __builtin_amdgcn_mfma_f32_16x16x32_bf16(__builtin_bit_cast(bf8, a), __builtin_bit_cast(bf8, b), c, 0, 0, 0); }
DEV f32x4 mfma16(v2u a, v2u b, f32x4 c) { return __builtin_amdgcn_mfma_f32_16x16x16bf16_1k(__builtin_bit_cast(s4, a), __builtin_bit_cast(s4, b), c, 0, 0, 0); }
DEV void mfma32t(f32x4& c, v4u a, v4u b) { asm volatile("v_mfma_f32_16x16x32_bf16 %0, %1, %2, %0" : "+v"(c) : "v"(a), "v"(b)); }
DEV void mfma32z(f32x4& c, v4u a, v4u b) { asm volatile("v_mfma_f32_16x16x32_bf16 %0, %1, %2, 0" : "=&v"(c) : "v"(a), "v"(b)); }
DEV u32 pk2(float a, float b) { bf2 p = {(__bf16)a, (__bf16)b}; return __builtin_bit_cast(u32, p); }
DEV v2u pk4(f32x4 v) { return v2u{pk2(v[0], v[1]), pk2(v[2], v[3])}; }
DEV float asf(u32 u) { return __builtin_bit_cast(float, u); }
DEV u32 asu(float f) { return __builtin_bit_cast(u32, f); }
DEV f32x4 unpk4(u32 lo, u32 hi) { return f32x4{asf(lo << 16), asf(lo & 0xffff0000u), asf(hi << 16), asf(hi & 0xffff0000u)}; }
DEV u32 uni(u32 v) { return __builtin_amdgcn_readfirstlane(v); }
DEV v4u lds128(u32 a) { return *(const LAS v4u*)a; }
DEV v2u lds64(u32 a) { return *(const LAS v2u*)a; }
DEV f32x4 ldsf4(u32 a) { return *(const LAS f32x4*)a; }
DEV f32x2 ldsf2(u32 a) { return *(const LAS f32x2*)a; }
DEV float ldsf(u32 a) { return *(const LAS float*)a; }
DEV v2u ldstr(u32 a) { return __builtin_bit_cast(v2u, __builtin_amdgcn_ds_read_tr16_b64_v4i16((LAS s4*)a)); }
DEV float vmax(float a, float b) { float r; asm("v_max_f32 %0, %1, %2" : "=v"(r) : "v"(a), "v"(b)); return r; }
#define MEMFENCE() asm volatile("" ::: "memory")
#define SB0() __builtin_amdgcn_sched_barrier(0)
#define BARRIER() do { SB0(); asm volatile("s_waitcnt lgkmcnt(0)\n\ts_barrier" ::: "memory"); SB0(); } while (0)
#define SGB(m, n) __builtin_amdgcn_sched_group_barrier(m, n, 0)
#define VMWAIT0() asm volatile("s_waitcnt vmcnt(0)" ::: "memory")
#define LGWAIT0() asm volatile("s_waitcnt lgkmcnt(0)" ::: "memory")
template <int N> DEV void vmwait() { asm volatile("s_waitcnt vmcnt(%0)" ::"i"(N) : "memory"); }
#define GLD "global_load_lds_dwordx4 "
DEV void dma3(const char* sb, u32 v, u32 dst) {
  asm volatile("s_mov_b32 m0, %2\n\ts_nop 0\n\t" GLD "%0, %1\n\t" GLD "%0, %1 offset:1024\n\t" GLD "%0, %1 offset:2048" ::"v"(v), "s"(sb), "s"(dst) : "memory");
}
DEV void dma2v(const char* sb, u32 v0, u32 v1, u32 dst) {
  asm volatile("s_mov_b32 m0, %3\n\ts_nop 0\n\t" GLD "%0, %2\n\t" GLD "%1, %2 offset:1024" ::"v"(v0), "v"(v1), "s"(sb), "s"(dst) : "memory");
}
DEV void dma3v(const char* sb, u32 v0, u32 v1, u32 v2, u32 dst) {
  asm volatile("s_mov_b32 m0, %4\n\ts_nop 0\n\t" GLD "%0, %3\n\t" GLD "%1, %3 offset:1024\n\t" GLD "%2, %3 offset:2048"
               ::"v"(v0), "v"(v1), "v"(v2), "s"(sb), "s"(dst) : "memory");
}
DEV void dma4v(const char* sb, u32 v0, u32 v1, u32 v2, u32 v3, u32 dst) {
  asm volatile("s_mov_b32 m0, %5\n\ts_nop 0\n\t" GLD "%0, %4\n\t" GLD "%1, %4 offset:1024\n\t" GLD "%2, %4 offset:2048\n\t" GLD "%3, %4 offset:3072"
               ::"v"(v0), "v"(v1), "v"(v2), "v"(v3), "s"(sb), "s"(dst) : "memory");
}
DEV void dma4w(const char* sb, u32 v0, u32 v1, u32 v2, u32 v3, u32 dst) {
  asm volatile("s_mov_b32 m0, %5\n\ts_nop 0\n\t" GLD "%0, %4\n\t" GLD "%1, %4 offset:1152\n\t" GLD "%2, %4 offset:2048\n\t" GLD "%3, %4 offset:3200"
               ::"v"(v0), "v"(v1), "v"(v2), "v"(v3), "s"(sb), "s"(dst) : "memory");
}
DEV void dma_more2048(const char* sb, u32 v) { asm volatile(GLD "%0, %1 offset:2048" ::"v"(v), "s"(sb) : "memory"); }
DEV void touch(const char* sb, u32 v) {
  asm volatile("s_mov_b32 m0, %2\n\ts_nop 0\n\tglobal_load_lds_dword %0, %1" ::"v"(v), "s"(sb), "s"(L_CTRL + 3072) : "memory");
}
DEV void dma1_sc1(const char* sb, u32 v, u32 dst) {
  asm volatile("s_mov_b32 m0, %2\n\ts_nop 0\n\t" GLD "%0, %1 sc1" ::"v"(v), "s"(sb), "s"(dst) : "memory");
}
DEV void dma4_sc1(const char* sb, u32 v, u32 dst) {
  asm volatile("s_mov_b32 m0, %2\n\ts_nop 0\n\t" GLD "%0, %1 sc1\n\t" GLD "%0, %1 offset:1024 sc1\n\t" GLD "%0, %1 offset:2048 sc1\n\t"
               GLD "%0, %1 offset:3072 sc1" ::"v"(v), "s"(sb), "s"(dst) : "memory");
}
DEV void dma8_sc1(const char* sb, u32 v, u32 dst) {
  asm volatile("s_mov_b32 m0, %2\n\ts_nop 0\n\t" GLD "%0, %1 offset:-4096 sc1\n\t" GLD "%0, %1 offset:-3072 sc1\n\t" GLD "%0, %1 offset:-2048 sc1\n\t"
               GLD "%0, %1 offset:-1024 sc1\n\t" GLD "%0, %1 sc1\n\t" GLD "%0, %1 offset:1024 sc1\n\t" GLD "%0, %1 offset:2048 sc1\n\t"
               GLD "%0, %1 offset:3072 sc1" ::"v"(v), "s"(sb), "s"(dst) : "memory");
}
DEV void dma2_sc1(const char* sb, u32 v, u32 dst) {
  asm volatile("s_mov_b32 m0, %2\n\ts_nop 0\n\t" GLD "%0, %1 sc1\n\t" GLD "%0, %1 offset:1024 sc1" ::"v"(v), "s"(sb), "s"(dst) : "memory");
}
DEV void dma_more2048_sc1(const char* sb, u32 v) { asm volatile(GLD "%0, %1 offset:2048 sc1" ::"v"(v), "s"(sb) : "memory"); }
DEV void lflag_set(u32 word, u32 v) {
  MEMFENCE();
  __hip_atomic_store((LAS u32*)(C_FLAGS + word * 4), v, __ATOMIC_RELAXED, __HIP_MEMORY_SCOPE_WORKGROUP);
  MEMFENCE();
}
DEV u32 lflag_min4(u32 word) {
  v4u v;
  asm volatile("ds_read_b128 %0, %1\n\ts_waitcnt lgkmcnt(0)" : "=v"(v) : "v"(C_FLAGS + word * 4) : "memory");
  u32 m = min(min(v.x, v.y), min(v.z, v.w));
  return uni(m);
}
DEV u32 lflag_get(u32 word) {
  u32 v;
  asm volatile("ds_read_b32 %0, %1\n\ts_waitcnt lgkmcnt(0)" : "=v"(v) : "v"(C_FLAGS + word * 4) : "memory");
  return uni(v);
}
DEV void lwait4(u32 word, u32 want) {
#pragma unroll 1
  for (u32 s = 0; s < SPIN_L && lflag_min4(word) < want; ++s) {} }
DEV void lwait1(u32 word, u32 want) {
#pragma unroll 1
  for (u32 s = 0; s < SPIN_L && lflag_get(word) < want; ++s) {} }
DEV void gflag_pub(char* gw, u32 off, u64 tag, int lane) {
  VMWAIT0();
  if (lane < FLW) __hip_atomic_store((GAS u64*)(gw + off + lane * 8), tag, __ATOMIC_RELAXED, __HIP_MEMORY_SCOPE_AGENT);
  MEMFENCE();
}
DEV u64 gflag_poll(char* gw, u32 off, u64 tag, int lane, int n) {
  u64 v = __hip_atomic_load((GAS u64*)(gw + off + (lane & 7) * FSP + ((lane & (n - 1)) >> 3) * (32 * FSP)), __ATOMIC_RELAXED, __HIP_MEMORY_SCOPE_AGENT);
  u64 m = __builtin_amdgcn_ballot_w64(v == tag);
  MEMFENCE();
  return m;
}
DEV bool vm_idle() { return (__builtin_amdgcn_s_getreg((31 << 11) | 7) & 0xC0000Fu) == 0; }
DEV u64 mix64(u64 z) {
  z += 0x9E3779B97F4A7C15ull; z = (z ^ (z >> 30)) * 0xBF58476D1CE4E5B9ull; z = (z ^ (z >> 27)) * 0x94D049BB133111EBull; return z ^ (z >> 31);
}
DEV float xg_sum(float v) {
  auto a = __builtin_amdgcn_permlane16_swap(asu(v), asu(v), false, false);
  v = asf(a[0]) + asf(a[1]);
  auto b = __builtin_amdgcn_permlane32_swap(asu(v), asu(v), false, false);
  return asf(b[0]) + asf(b[1]);
}
template <int CTRL> DEV float dppf(float v) { return asf(__builtin_amdgcn_update_dpp(0u, asu(v), CTRL, 0xf, 0xf, false)); }
DEV void ln_coef(float s, float q, float& r, float& b) {
  float m = s * (1.0f / 512.0f);
  float var = fmaxf(q * (1.0f / 512.0f) - m * m, 0.0f);
  r = __builtin_amdgcn_rsqf(var + 1e-5f);
  b = -m * r;
}
#define STAMP(i)
#define CSTAMP(i)
#define STAMP_DECL
#define STAMP_OUT()

struct Frag4 { v4u f[4]; };
DEV void wcell_dma_half(const char* src, u32 ve, u32 cell, int h) { dma2v(src + h * 8192, ve, (ve ^ 64) + 3072, cell + h * 2048); }
DEV u32 wcell_voff(int lane, int j) {
  const int r = lane >> 3;
  return (16 * (r >> 2) + (r & 3)) * 1024 + j * 128 + ((((lane & 7) >> 1) ^ ((r >> 1) & 1)) * 32) + (lane & 1) * 16;
}
DEV u32 wcell_roff(int lane) {
  const int G = lane >> 4, q = (lane & 15) >> 2;
  return G * 1024 + q * 128 + ((((q >> 1) & 1) | ((G & 1) << 1)) * 32) + (lane & 3) * 8;
}
DEV v4u trfrag(u32 ad, u32 d2) { v2u lo = ldstr(ad), hi = ldstr(ad + d2); return v4u{lo.x, lo.y, hi.x, hi.y}; }
DEV v4u act_frag(u32 a, u32 a2, u32 off) { v2u lo = lds64(a + off), hi = lds64(a2 + off); return v4u{lo.x, lo.y, hi.x, hi.y}; }
DEV void dma1(const char* sb, u32 v, u32 dst) { asm volatile("s_mov_b32 m0, %2\n\ts_nop 0\n\t" GLD "%0, %1" ::"v"(v), "s"(sb), "s"(dst) : "memory"); }
DEV void dma5_sc1(const char* sb, u32 v, u32 dst) {
  asm volatile("s_mov_b32 m0, %2\n\ts_nop 0\n\t" GLD "%0, %1 offset:-1024 sc1\n\t" GLD "%0, %1 sc1\n\t" GLD "%0, %1 offset:1024 sc1\n\t"
               GLD "%0, %1 offset:2048 sc1\n\t" GLD "%0, %1 offset:3072 sc1" ::"v"(v), "s"(sb), "s"(dst) : "memory");
}
DEV void dma_morem2048_sc1(const char* sb, u32 v) { asm volatile(GLD "%0, %1 offset:-2048 sc1" ::"v"(v), "s"(sb) : "memory"); }
DEV void touch64(const char* ad) { asm volatile("s_mov_b32 m0, %1\n\ts_nop 0\n\tglobal_load_lds_dword %0, off sc1" ::"v"(ad), "s"(L_CTRL + 3072) : "memory"); }
DEV void poll8(char* gw, u32 off, u64 tag, int lane) {
#pragma unroll 1
  for (u32 s = 0; s < SPIN_G && (gflag_poll(gw, off, tag, lane, 8) & 0xff) != 0xff; ++s) {}
}

constexpr int SCH[17][4] = {{1, 2, 3, 4},     {5, 18, -1, -1},  {6, 19, -1, -1},  {7, 20, -1, -1},  {8, 21, -1, -1},  {9, 22, -1, -1},
                            {10, 23, -1, -1}, {11, -1, -1, -1}, {12, -1, -1, -1}, {13, -1, -1, -1}, {14, -1, -1, -1}, {15, -1, -1, -1},
                            {24, -1, -1, -1}, {25, -1, -1, -1}, {26, -1, -1, -1}, {27, -1, -1, -1}, {30, 31, -1, -1}};
constexpr int item_ops(int it) { return it < 0 ? 0 : it < 16 ? 3 : it < 24 ? ((it & 1) ? 2 : 3) : it < 30 ? 4 : 3; }
constexpr int item_end(int it) { int n = 0; for (int gI = 0; gI < 17; ++gI) for (int k = 0; k < 4; ++k) { n += item_ops(SCH[gI][k]); if (SCH[gI][k] == it) return n; } return 0; }
constexpr int issued_before_barrier(int b) { int n = 0; for (int gI = 0; gI <= b; ++gI) for (int k = 0; k < 4; ++k) n += item_ops(SCH[gI][k]); return n; }
constexpr int xpair_at(int b) { return b < 4 ? 0 : b < 7 ? 1 : b < 9 ? 2 : 3; }
constexpr int need_before_barrier(int b) { return item_end(b) > item_end(17 + 2 * xpair_at(b)) ? item_end(b) : item_end(17 + 2 * xpair_at(b)); }

__global__ void __launch_bounds__(512, 2) fused_block(Params p) {
  extern __shared__ __attribute__((aligned(16))) char smem[];
  const int tid = threadIdx.x, lane = tid & 63;
  const int w = uni(tid >> 6), lo = w & 3, hi = w >> 2;
  const int bid = blockIdx.x, xcd = bid & 7, idx = bid >> 3;
  const int g = xcd * 4 + (idx >> 3), j = idx & 7;
  const int T0 = g * 64;
  const int t = lane & 15, G = lane >> 4, rho = lane >> 3;
  char* const gw = p.ws + group_base(g);
  const __amdgpu_buffer_rsrc_t rws = __builtin_amdgcn_make_buffer_rsrc((void*)gw, 0, G_BYTES, 0x00020000);
  STAMP_DECL;
  STAMP(0); CSTAMP(10);
  asm volatile("" ::"s"(smem));
  const u32 wv = (8 * lo + rho) * 3072 + j * 128 + (((((lane & 7) >> 1) ^ (((rho >> 1) & 1) | ((lo & 1) << 1))) * 32) + (lane & 1) * 16);
  auto wload = [&](int c) { dma3(p.wqkv + (size_t)((c + 2 * xcd) & 15) * (32 * 3072), wv, L_RING + (c % NSLOT) * SLOT + lo * 3072); };
  u32 zz = 0; asm volatile("" : "+v"(zz));
  u64 epv;
  asm volatile("global_load_dwordx2 %0, %1, %2 sc1" : "=v"(epv) : "v"(zz), "s"(gw + G_EPOCH) : "memory");
  if (hi == 0) wload(0); else { wload(1); wload(2); wload(3); wload(4); }
  u32 xv[5];
#pragma unroll
  for (int n = 0; n < 5; ++n) {
    const int i = (5 * lo + n) % 10, row = T0 - 16 + 8 * i + rho;
    xv[n] = max(row, 0) * 1024 + ((((lane & 7) ^ (rho >> 1)) << 4) ^ ((i & 1) * 64)) + 2048 - (n < 3 ? n : n - 3) * 1024;
  }
  auto xload = [&](int P, int part) {
    const char* src = p.x - 2048 + ((2 * P + (lo >> 1) + xcd) & 7) * 128;
    if (part == 0) dma3v(src, xv[0], xv[1], xv[2], L_X + P * 20480 + 5 * lo * 1024);
    else dma2v(src, xv[3], xv[4], L_X + P * 20480 + (5 * lo + 3) * 1024);
  };
  if (hi == 0) { xload(0, 0); xload(0, 1); }
  const v4u ones = {0x3F803F80u, 0x3F803F80u, 0x3F803F80u, 0x3F803F80u};
  const f32x4 zero4 = {0, 0, 0, 0};
  const int Tj = 22 * (j >> 1) + ((j & 1) ? (j < 6 ? 12 : 10) : 0);
  const bool full = !(j & 1) && j < 6;
  const int lt = (full || lo < 3) ? 3 * lo : 8;
  const int nv = (full || lo < 2) ? 3 : 2;
  const u32 wev = wcell_voff(lane, j), ro = wcell_roff(lane);
  u32 w1v[3], ra[3];
  {
    const int r8 = (lane >> 2) & 7, mis = (r8 + (Tj >> 1)) & 1;
#pragma unroll
    for (int n = 0; n < 3; ++n) {
      int q6 = (2 * n + (lane >> 5) + 6 - mis) % 6;
      if (!full && q6 == 5) q6 = mis ? 0 : 4;
      w1v[n] = (16 * (r8 >> 2) + (r8 & 3) + 4 * lo) * 2752 + (Tj + 2 * q6 + (((lane >> 1) ^ lo) & 1)) * 32 + (lane & 1) * 16 + 2048 - n * 1024;
      ra[n] = L_RING + G * 3072 + ((((lt + n) >> 1) + mis) % 6) * 512 + ((lane & 15) >> 2) * 64 + ((((lt + n) ^ G) & 1) * 32) + (lane & 3) * 8;
      asm volatile("" : "+v"(ra[n]));
    }
  }
  auto w1load = [&](int ks, int sl) { dma3v(p.w1 - 2048 + (size_t)(32 * ks) * 2752, w1v[0], w1v[1], w1v[2], L_RING + sl * SLOT + lo * 3072); };
  v4u w1[8][3];
  auto w1read = [&](int r, int sl) {
#pragma unroll
    for (int u = 0; u < 3; ++u) w1[r][u] = trfrag(ra[u] + sl * SLOT, 256);
  };
  const char* w2s = p.w2 + (size_t)(11 * lo) * 32768;
  Frag4 wo[4];
  auto woread = [&]() {
#pragma unroll
    for (int s = 0; s < 4; ++s)
#pragma unroll
      for (int c = 0; c < 4; ++c) wo[s].f[c] = trfrag(s * 16384 + lo * 4096 + (ro ^ (c * 32)), 512);
  };
  v2u xres[2];
  const u32 xra = L_X + (((j - xcd) & 7) * 5 + lo + 1) * 2048 + (t >> 3) * 1024 + (t & 7) * 128 + (((4 * hi + (G >> 1)) ^ ((t >> 1) & 7)) << 4) + (G & 1) * 8;
  const u32 xl = (t >> 3) * 1024 + (t & 7) * 128 + ((G ^ ((t >> 1) & 7)) << 4);
  u64 tag;
  int hr_ = hi;
  asm volatile("" : "+s"(hr_));

  if (hr_ == 0) {
    __builtin_amdgcn_s_setprio(2);
    if (w == 0 && lane < 8) *(LAS u32*)(C_FLAGS + lane * 4) = 0;
    f32x4 aq[5], ak[5], av[5];
#pragma unroll
    for (int i = 0; i < 5; ++i) { aq[i] = zero4; ak[i] = zero4; av[i] = zero4; asm volatile("" : "+v"(aq[i]), "+v"(ak[i]), "+v"(av[i])); }
    const int q = (lane & 15) >> 2;
    const u32 woff = L_RING + G * 3072 + q * 128 + ((lo ^ (((q >> 1) & 1) | ((G & 1) << 1))) * 32) + (lane & 3) * 8;
    struct Fr { v4u w[3]; v4u x[5]; };
    auto ldfr = [&](Fr& f, u32 wa, u32 xa) {
#pragma unroll
      for (int m = 0; m < 3; ++m) f.w[m] = trfrag(wa + m * 1024, 512);
#pragma unroll
      for (int i = 0; i < 5; ++i) f.x[i] = lds128(xa + i * 2048);
    };
    auto mma = [&](Fr& f) {
      v4u x0 = f.x[0];
      if (t == 0) x0 = ones;
#pragma unroll
      for (int i = 1; i < 5; ++i) { aq[i] = mfma32(f.w[0], f.x[i], aq[i]); ak[i] = mfma32(f.w[1], f.x[i], ak[i]); av[i] = mfma32(f.x[i], f.w[2], av[i]); }
      aq[0] = mfma32(f.w[0], x0, aq[0]); ak[0] = mfma32(f.w[1], x0, ak[0]); av[0] = mfma32(x0, f.w[2], av[0]);
    };
    auto pat = [&]() {
#pragma unroll
      for (int i = 0; i < 6; ++i) { SGB(0x8, 1); SGB(0x100, 2); }
      SGB(0x8, 9);
    };
    Fr f0, f1;
    u32 so = 0, xa = L_X + xl;
    VMWAIT0();
    BARRIER();
    STAMP(1); CSTAMP(24);
    ldfr(f0, woff, xa);
    asm volatile("" : "+v"(epv));
    tag = mix64(((u64)uni((u32)(epv >> 32)) << 32) | uni((u32)epv));
    SB0();
#pragma unroll 1
    for (int it = 0; it < 7; ++it) {
      so = (so == (NSLOT - 1) * SLOT) ? 0 : so + SLOT;
      BARRIER();
      ldfr(f1, woff + so, xa ^ 64);
      mma(f0);
      pat();
      so = (so == (NSLOT - 1) * SLOT) ? 0 : so + SLOT;
      xa += 10240;
      BARRIER();
      ldfr(f0, woff + so, xa);
      mma(f1);
      pat();
      SB0();
      if (it <= 4) { xres[0] = lds64(xra); xres[1] = lds64(xra ^ 32); }
      if (it == TW) {
        const u32 id = bid * 102 + min(lo * 26 + (lane < 26 ? lane : 25), 101);
        touch64(id < 4096 ? p.wo + id * 128 : id < 15104 ? p.w1 + (id - 4096) * 128 : p.w2 + (id - 15104) * 128);
      }
      if (it == TO) {
        if (lane < 8) touch64(gw + G_FLA + (lane * 32 + j * 4 + lo) * FSP);
      }
    }
    BARRIER();
    ldfr(f1, woff + 3 * SLOT, xa ^ 64);
    f32x2 rb1[5]; f32x4 rbA[5], rbB[5];
    {
      u32 ca = C_R + t * 8, cb = C_R + G * 32;
      asm volatile("" : "+v"(ca), "+v"(cb));
#pragma unroll
      for (int i = 0; i < 5; ++i) { rb1[i] = ldsf2(ca + i * 128); rbA[i] = ldsf4(cb + i * 128); rbB[i] = ldsf4(cb + i * 128 + 16); }
    }
    mma(f0);
#pragma unroll
    for (int i = 0; i < 13; ++i) { SGB(0x8, 1); SGB(0x100, 2); }
    SGB(0x8, 2);
    BARRIER();
    {
      v4u x0 = f1.x[0];
      if (t == 0) x0 = ones;
      aq[0] = mfma32(f1.w[0], x0, aq[0]); ak[0] = mfma32(f1.w[1], x0, ak[0]); av[0] = mfma32(x0, f1.w[2], av[0]);
#pragma unroll
      for (int i = 1; i < 5; ++i) { ak[i] = mfma32(f1.w[1], f1.x[i], ak[i]); aq[i] = mfma32(f1.w[0], f1.x[i], aq[i]); }
#pragma unroll
      for (int i = 1; i < 5; ++i) av[i] = mfma32(f1.x[i], f1.w[2], av[i]);
    }
    STAMP(2); CSTAMP(25);
    v2u kb[5], vb[5], qb[5];
    f32x4 csq, csk;
#pragma unroll
    for (int r = 0; r < 4; ++r) { csq[r] = dppf<0x150>(aq[0][r]); csk[r] = dppf<0x150>(ak[0][r]); }
    const float csv = __shfl(av[0][0], t);
#pragma unroll
    for (int i = 0; i < 5; ++i) {
      kb[i] = pk4(ak[i] * rb1[i][0] + csk * rb1[i][1]);
      if (i > 0) qb[i] = pk4(aq[i] * rb1[i][0] + csq * rb1[i][1]);
    }
    SB0();
    f32x4 sp[4], sc[4];
#pragma unroll
    for (int i = 0; i < 4; ++i) { sp[i] = mfma16(kb[i], qb[i + 1], zero4); sc[i] = mfma16(kb[i + 1], qb[i + 1], zero4); }
#pragma unroll
    for (int i = 0; i < 5; ++i)
      vb[i] = v2u{pk2(av[i][0] * rbA[i][0] + csv * rbA[i][1], av[i][1] * rbA[i][2] + csv * rbA[i][3]),
                  pk2(av[i][2] * rbB[i][0] + csv * rbB[i][1], av[i][3] * rbB[i][2] + csv * rbB[i][3])};
    SB0();
    const bool par = t & 1;
    const bool cur0 = 4 * G + (t & 1) <= t, cur1 = 4 * G + (t & 1) + 2 <= t;
    const u32 sh = (t & 1) * 16;
    float z0[4], z1[4], m[4];
#pragma unroll
    for (int i = 0; i < 4; ++i) {
      const float c0 = par ? sc[i][1] : sc[i][0], c1 = par ? sc[i][3] : sc[i][2];
      float p0 = par ? sp[i][1] : sp[i][0], p1 = par ? sp[i][3] : sp[i][2];
      if (i == 0 && g == 0) { p0 = -4e9f; p1 = -4e9f; }
      z0[i] = cur0 ? c0 : p0; z1[i] = cur1 ? c1 : p1;
      m[i] = vmax(z0[i], z1[i]);
    }
    SB0();
#pragma unroll
    for (int i = 0; i < 4; ++i) { auto s_ = __builtin_amdgcn_permlane16_swap(asu(m[i]), asu(m[i]), false, false); m[i] = vmax(asf(s_[0]), asf(s_[1])); }
    SB0();
#pragma unroll
    for (int i = 0; i < 4; ++i) { auto s_ = __builtin_amdgcn_permlane32_swap(asu(m[i]), asu(m[i]), false, false); m[i] = vmax(asf(s_[0]), asf(s_[1])); }
    SB0();
    f32x4 o[4], den[4];
#pragma unroll
    for (int i = 0; i < 4; ++i) {
      const float e0 = __builtin_amdgcn_exp2f((z0[i] - m[i]) * 0.36067376022f), e1 = __builtin_amdgcn_exp2f((z1[i] - m[i]) * 0.36067376022f);
      const u32 u = pk2(e0, e1);
      const u32 w0 = (u & 0xffffu) << sh, w1_ = (u >> 16) << sh;
      const v4u P = {cur0 ? 0u : w0, cur1 ? 0u : w1_, cur0 ? w0 : 0u, cur1 ? w1_ : 0u};
      const v4u V = {vb[i].x, vb[i].y, vb[i + 1].x, vb[i + 1].y};
      den[i] = mfma32(ones, P, zero4);
      o[i] = mfma32(V, P, zero4);
    }
    SB0();
#pragma unroll
    for (int i = 0; i < 4; ++i) {
      const v2u ob = pk4(o[i] * __builtin_amdgcn_rcpf(den[i][0]));
      __builtin_amdgcn_raw_buffer_store_b64(ob, rws, lane * 8, G_O + (i * 32 + 4 * j + lo) * 512, 16);
    }
    SB0();
    STAMP(23);
    woread();
    gflag_pub(gw, G_FLA + (j * 4 + lo) * FSP, tag, lane);
    STAMP(3);
    __builtin_amdgcn_s_setprio(0);
#pragma unroll
    for (int r = 0; r < 6; ++r)
#pragma unroll
      for (int u = 0; u < 3; ++u) w1[r][u] = v4u{0, 0, 0, 0};
  } else {
    __builtin_amdgcn_s_setprio(3);
    auto item = [&](auto I) {
      constexpr int it = decltype(I)::value;
      if constexpr (it >= 30) w1load(it - 30, (it - 30 + 4) % 6);
      else if constexpr (it >= 24) { wcell_dma_half(p.wo + (size_t)(4 * lo + it - 24) * 32768, wev, (it - 24) * 16384 + lo * 4096, 0); wcell_dma_half(p.wo + (size_t)(4 * lo + it - 24) * 32768, wev, (it - 24) * 16384 + lo * 4096, 1); }
      else if constexpr (it >= 16) xload((it - 16) / 2, it & 1);
      else if constexpr (it >= 0) wload(it);
    };
    const u32 xo0 = L_X + lo * 2048 + xl;
    const int t4 = 4 * lo + (t & 3);
    const u32 x4 = L_X + ((t >> 3) * 5 + 4) * 2048 + (t4 >> 3) * 1024 + (t4 & 7) * 128 + ((((t >> 2) & 1) * 4 + G) << 4);
    f32x4 s0 = zero4, q0 = zero4, s4 = zero4, q4 = zero4;
    v4u xs[2][3];
    float sq4 = 0.f;
    auto diag = [&](f32x4 v) { const float l_ = (t & 1) ? v[1] : v[0], h_ = (t & 1) ? v[3] : v[2]; return (t & 2) ? h_ : l_; };
    auto step = [&](auto I) {
      constexpr int be = decltype(I)::value;
      vmwait<issued_before_barrier(be) - need_before_barrier(be)>();
      BARRIER();
      if constexpr (be >= 4 && be <= 10) {
#pragma unroll
        for (int k = 0; k < 3; ++k) {
          const int i = 3 * (be - 4) + k, P = i / 5, kk = i % 5;
          if (i < 20) xs[be & 1][k] = kk < 4 ? lds128((xo0 ^ ((kk & 1) * 64)) + (2 * P + (kk >> 1)) * 10240) : lds128(x4 + P * 20480);
        }
      }
      if constexpr (be == 10) { xres[0] = lds64(xra); xres[1] = lds64(xra ^ 32); }
      SB0();
      item(IC(SCH[be + 1][0])); item(IC(SCH[be + 1][1]));
      SB0();
      if constexpr (be >= 5 && be <= 11) {
#pragma unroll
        for (int k = 0; k < 3; ++k) {
          constexpr int i0 = 3 * (be - 5);
          if (i0 + k < 20) {
            const v4u xx = xs[(be - 1) & 1][k];
            if ((i0 + k) % 5 < 4) { s0 = mfma32(ones, xx, s0); q0 = mfma32(xx, xx, q0); }
            else { s4 = mfma32(ones, xx, s4); q4 = mfma32(xx, xx, q4); }
          }
        }
      }
      if constexpr (be == 2) { asm volatile("" : "+v"(epv)); tag = mix64(((u64)uni((u32)(epv >> 32)) << 32) | uni((u32)epv)); }
      if constexpr (be == 12) {
        float r, bb;
        ln_coef(s0[0], diag(q0), r, bb);
        if (G == (t >> 2)) *(LAS f32x2*)(C_R + (16 * lo + t) * 8) = f32x2{r, bb};
        sq4 = xg_sum(G == (t >> 2) ? diag(q4) : 0.f);
      }
      if constexpr (be == 13) {
        float r2, b2, sm = s4[0], sq = sq4;
        sm += dppf<0x128>(sm); sq += dppf<0x128>(sq); sm += dppf<0x124>(sm); sq += dppf<0x124>(sq);
        ln_coef(sm, sq, r2, b2);
        if (lane < 4) *(LAS f32x2*)(C_R + (64 + t4) * 8) = f32x2{r2, b2};
      }
    };
    UNROLL_SEQ(step, 16);
    vmwait<6>();
    __builtin_amdgcn_s_setprio(1);
    BARRIER();
    STAMP(2);
#pragma unroll
    for (int ks = 2; ks < 6; ++ks) w1load(ks, (ks + 4) % 6);
    woread();
    VMWAIT0();
    lflag_set(FL_W1L + lo, 1);
    lwait4(FL_W1L, 1);
#pragma unroll
    for (int r = 0; r < 6; ++r) w1read(r, (r + 4) % 6);
    lflag_set(FL_W1C + lo, 1);
    wcell_dma_half(w2s, wev, L_W2 + lo * 4096, 0); wcell_dma_half(w2s, wev, L_W2 + lo * 4096, 1);
    lwait4(FL_W1C, 1);
    STAMP(23);
#pragma unroll
    for (int ks = 6; ks < 12; ++ks) w1load(ks, ks - 6);
    STAMP(3);
    __builtin_amdgcn_s_setprio(0);
  }
  if (P1ONLY) { VMWAIT0(); STAMP(8); STAMP_OUT(); return; }
  BARRIER();
  v4u w2[11][2];
  const u32 r2a = lo * 4096 + (ro ^ (hi * 64)), r2b = r2a ^ 32;
  auto w2read = [&](int s, u32 row) { w2[s][0] = trfrag(r2a + row, 512); w2[s][1] = trfrag(r2b + row, 512); };
  auto w2half = [&](int s, u32 row, int h) { wcell_dma_half(w2s + ((s == 10 && lo == 3) ? 9 : s) * 32768, wev, row + lo * 4096, h); };
  v4u hres;
  {
    poll8(gw, G_FLA + lo * 8 * FSP, tag, lane);
    STAMP(4);
    const u32 c0 = hi * 32768 + lo * 4096;
#pragma unroll
    for (int i = 0; i < 2; ++i) dma4_sc1(gw + G_O + c0 + i * 16384, lane * 16, c0 + i * 16384);
    BARRIER();
    if (hi) { w1read(6, 0); w1read(7, 1); } else { w1read(0, 2); w1read(1, 3); w1read(2, 4); w1read(3, 5); }
    w2read(0, L_W2);
    v4u bf[2][4];
    f32x4 acc[2][4];
    u32 oa = c0 + lane * 8, ob = oa + 512;
    asm volatile("" : "+v"(oa), "+v"(ob));
    vmwait<4>();
    BARRIER();
    STAMP(9);
#pragma unroll
    for (int s = 0; s < 4; ++s) bf[0][s] = act_frag(oa, ob, s * 1024);
    SB0();
#pragma unroll
    for (int s = 0; s < 2; ++s)
#pragma unroll
      for (int c = 0; c < 4; ++c) acc[0][c] = mfma32(wo[s].f[c], bf[0][s], s == 0 ? zero4 : acc[0][c]);
    SB0();
    VMWAIT0();
#pragma unroll
    for (int s = 0; s < 4; ++s) bf[1][s] = act_frag(oa, ob, 16384 + s * 1024);
    SB0();
#pragma unroll
    for (int s = 2; s < 4; ++s)
#pragma unroll
      for (int c = 0; c < 4; ++c) acc[0][c] = mfma32(wo[s].f[c], bf[0][s], acc[0][c]);
    SB0();
    w1load(12 + 2 * hi, 2 * hi);
    SB0();
#pragma unroll
    for (int s = 0; s < 2; ++s)
#pragma unroll
      for (int c = 0; c < 4; ++c) acc[1][c] = mfma32(wo[s].f[c], bf[1][s], s == 0 ? zero4 : acc[1][c]);
#pragma unroll
    for (int c = 0; c < 4; ++c) *(LAS f32x4*)(c0 + c * 1024 + lane * 16) = acc[0][c];
    SB0();
    w1load(13 + 2 * hi, 2 * hi + 1);
    SB0();
#pragma unroll
    for (int s = 2; s < 4; ++s)
#pragma unroll
      for (int c = 0; c < 4; ++c) acc[1][c] = mfma32(wo[s].f[c], bf[1][s], acc[1][c]);
#pragma unroll
    for (int c = 0; c < 4; ++c) *(LAS f32x4*)(c0 + 16384 + c * 1024 + lane * 16) = acc[1][c];
    STAMP(16);
    BARRIER();
    STAMP(17);
    const u32 pb = lo * 16384 + hi * 2048 + lane * 16;
    f32x4 hv[2];
#pragma unroll
    for (int c = 0; c < 2; ++c)
      hv[c] = ((ldsf4(pb + c * 1024) + ldsf4(pb + 4096 + c * 1024)) + (ldsf4(pb + 8192 + c * 1024) + ldsf4(pb + 12288 + c * 1024))) + unpk4(xres[c].x, xres[c].y);
    const v2u h0 = pk4(hv[0]), h1 = pk4(hv[1]);
    hres = v4u{h0.x, h0.y, h1.x, h1.y};
    __builtin_amdgcn_raw_buffer_store_b128(hres, rws, lane * 16, G_H + (lo * 16 + 2 * j + hi) * 1024, 16);
    SB0();
    const f32x4 s4 = hv[0] + hv[1], q4 = hv[0] * hv[0] + hv[1] * hv[1];
    const float sm = xg_sum((s4[0] + s4[1]) + (s4[2] + s4[3])), sq = xg_sum((q4[0] + q4[1]) + (q4[2] + q4[3]));
    if (lane < 16) __builtin_amdgcn_raw_buffer_store_b64(v2u{asu(sm), asu(sq)}, rws, lane * 8, G_ST + (j >> 1) * 2048 + lo * 512 + ((j & 1) * 2 + hi) * 128, 16);
    SB0();
  }
  STAMP(20);
  w2half(1, L_W2, hi); w2half(2, L_RING + 4 * SLOT, hi);
  f32x4 cs[3] = {zero4, zero4, zero4};
  auto w1sum = [&](int r) {
#pragma unroll
    for (int u = 0; u < 3; ++u) cs[u] = mfma32(w1[r][u], ones, cs[u]);
  };
  w1sum(0); w1sum(1); w1sum(2); w1sum(3);
  vmwait<4>();
  if (lane < FLW) __hip_atomic_store((GAS u64*)(gw + G_FLB + ((lo & 1) * 32 + j * 4 + (lo >> 1) * 2 + hi) * FSP + lane * 8), tag, __ATOMIC_RELAXED, __HIP_MEMORY_SCOPE_AGENT);
  MEMFENCE();
  STAMP(5);
  vmwait<1>();
  BARRIER();
  if (hi == 0) { w1read(4, 0); w1read(5, 1); w1read(6, 2); w1read(7, 3); }
  w2read(1, L_W2); w2read(2, L_RING + 4 * SLOT);
  BARRIER();
#pragma unroll
  for (int s = 0; s < 5; ++s) w2half(3 + s, L_W2 + s * 16384, hi);
  w1sum(4); w1sum(5); w1sum(6); w1sum(7);
  poll8(gw, G_FLB + (hi * 32 + lo * 8) * FSP, tag, lane);
  STAMP(6);
  dma4_sc1(gw + G_H + (hi * 16 + 4 * lo) * 1024, lane * 16, hi * 16384 + lo * 4096);
  dma4_sc1(gw + G_H + ((hi + 2) * 16 + 4 * lo) * 1024, lane * 16, (hi + 2) * 16384 + lo * 4096);
  BARRIER();
  dma1_sc1(gw + G_ST + w * 1024, lane * 16, C_S2 + w * 1024);
#pragma unroll
  for (int s = 0; s < 5; ++s) w2read(3 + s, L_W2 + s * 16384);
  STAMP(21);
  if (t == 0) {
#pragma unroll
    for (int u = 0; u < 3; ++u) *(LAS f32x4*)(C_CS1 + (w * 3 + u) * 64 + G * 16) = cs[u];
  }
  vmwait<5>();
  BARRIER();
  STAMP(7); CSTAMP(29);

  const u32 fco = G_FLC + (j * 4 + lo) * FSP;
  auto fpub = [&](int i) { if (lane < FLW) __hip_atomic_store((GAS u64*)(gw + fco + i * FBL + lane * 8), tag, __ATOMIC_RELAXED, __HIP_MEMORY_SCOPE_AGENT); MEMFENCE(); };
  auto fhalf = [&](int i, u32 slot, int h) {
    const char* src = gw + G_F + i * F_TAU + lo * FPART;
    const u32 dst = slot + lo * FPART;
    if (h == 0) { dma5_sc1(src + 2048, lane * 16, dst + 2048); dma_morem2048_sc1(src + 2048, lane * 16); }
    else if (lo < 3) dma5_sc1(src + 7168, lane * 16, dst + 7168);
    else dma4_sc1(src + 6144, lane * 16, dst + 6144);
  };
  {
    constexpr int PD = 3;
    v4u hf[32 + PD];
    f32x4 acc[4][3], pv[3], c4[3];
    f32x2 sa[2][4];
    float rr[2], bb[2];
    u32 hA = (1 - hi) * (8192 + 16384) + lane * 16, hB = (1 - hi) * 8192 + hi * 16384 + lane * 16;
    u32 pxw = w * 3072 + lane * 16, pxr = (w ^ 4) * 3072 + lane * 16;
    u32 csa = C_CS1 + lo * 192 + G * 16, sta = C_S2 + G * 2048 + hi * 512 + t * 8;
    asm volatile("" : "+v"(hA), "+v"(hB), "+v"(csa), "+v"(sta));
    auto hrd = [&](int n) { return lds128((((n >> 3) & 1) ? hB : hA) + (n >> 4) * 32768 + (n & 7) * 1024); };
    auto ep_load = [&](int sbk) {
      const u32 pxa = pxr + (sbk ? 0 : L_P3);
      if (sbk == 0) {
#pragma unroll
        for (int k = 0; k < 4; ++k) sa[0][k] = ldsf2(sta + k * 128);
      }
#pragma unroll
      for (int u = 0; u < 3; ++u) pv[u] = ldsf4(pxa + u * 1024);
    };
    auto ep_coef = [&](int k) { const f32x2 a_ = (sa[k][0] + sa[k][1]) + (sa[k][2] + sa[k][3]); ln_coef(xg_sum(a_[0]), xg_sum(a_[1]), rr[k], bb[k]); };
    auto ep_out = [&](int sbk, int u) {
      f32x4 v = (acc[2 * sbk + 1][u] + pv[u]) * rr[sbk] + c4[u] * bb[sbk];
      const v2u fo = {pk2(fmaxf(v[0], 0.f), fmaxf(v[1], 0.f)), pk2(fmaxf(v[2], 0.f), fmaxf(v[3], 0.f))};
      if (u < 2 || nv == 3) __builtin_amdgcn_raw_buffer_store_b64(fo, rws, lane * 8, G_F + (2 * sbk + hi) * F_TAU + (Tj + lt + u) * 512, 16);
    };
#pragma unroll
    for (int k = 0; k < PD; ++k) hf[k] = hrd(k);
    auto p3step = [&](auto I) {
      constexpr int n = decltype(I)::value, i = n >> 3, r = n & 7;
      acc[i][0] = mfma32(w1[r][0], hf[n], r == 0 ? zero4 : acc[i][0]);
      SB0();
      if constexpr (n + PD < 32 && !(n < 16 && n + PD >= 16)) hf[n + PD] = hrd(n + PD);
      if constexpr (n == 1 || n == 4 || n == 7) w2half(8 + n / 3, L_W2 + (n / 3) * 16384, hi);
      if constexpr (n == 12) {
#pragma unroll
        for (int u = 0; u < 3; ++u) c4[u] = ldsf4(csa + u * 64) + ldsf4(csa + 768 + u * 64);
      }
      if constexpr (n == 10 || n == 26) {
#pragma unroll
        for (int u = 0; u < 3; ++u) *(LAS f32x4*)(pxw + (n == 10 ? L_P3 : 0) + u * 1024) = acc[i - 1][u];
      }
      if constexpr (n == 17) ep_coef(0);
      if constexpr (n >= 18 && n <= 20) ep_out(0, n - 18);
      if constexpr (n == 21) {
#pragma unroll
        for (int k = 0; k < 4; ++k) sa[1][k] = ldsf2(sta + 1024 + k * 128);
      }
      if constexpr (n == 23) ep_coef(1);
      if constexpr (n == 27 || n == 28 || n == 30) w2read(8 + (n == 27 ? 0 : n == 28 ? 1 : 2), L_W2 + (n == 27 ? 0 : n == 28 ? 1 : 2) * 16384);
      if constexpr (n == 29) { VMWAIT0(); fpub(hi); }
      SB0();
      acc[i][1] = mfma32(w1[r][1], hf[n], r == 0 ? zero4 : acc[i][1]);
      acc[i][2] = mfma32(w1[r][2], hf[n], r == 0 ? zero4 : acc[i][2]);
      SB0();
      if constexpr (n == 15) {
        VMWAIT0();
        BARRIER();
        STAMP(26); CSTAMP(30);
#pragma unroll
        for (int k = 0; k < PD; ++k) hf[16 + k] = hrd(16 + k);
        ep_load(0);
        SB0();
      }
    };
    UNROLL_SEQ(p3step, 32);
    BARRIER();
    STAMP(22);
    ep_load(1);
#pragma unroll
    for (int u = 0; u < 3; ++u) ep_out(1, u);
  }
  STAMP(18);
  {
    bool pub = false;
#pragma unroll 1
    for (u32 s = 0; s < SPIN_G; ++s) {
      const u32 m = (u32)gflag_poll(gw, G_FLC + hi * FBL + lo * 8 * FSP, tag, lane, 8);
      if (!pub) { VMWAIT0(); fpub(2 + hi); pub = true; STAMP(19); }
      if ((m & 0xff) == 0xff) break;
    }
    fhalf(hi, hi ? L_F2 : L_F1, 0); fhalf(hi, hi ? L_F2 : L_F1, 1);
    STAMP(14);
    f32x4 acc[4][2];
    auto tile = [&](int i, u32 slot) {
      u32 sa = slot + lo * FPART + lane * 8, sb = sa + 512;
      asm volatile("" : "+v"(sa), "+v"(sb));
      constexpr int PD = 4;
      v4u bf[11];
      SB0();
#pragma unroll
      for (int k = 0; k < PD; ++k) bf[k] = act_frag(sa, sb, k * 1024);
      const u32 mk = (i == lo) ? 0xffffffffu : 0u;
      acc[i][0] = unpk4(hres.x & mk, hres.y & mk); acc[i][1] = unpk4(hres.z & mk, hres.w & mk);
      SB0();
#pragma unroll
      for (int k = 0; k < 10; ++k) {
        acc[i][0] = mfma32(w2[k][0], bf[k], acc[i][0]);
        SB0();
        if (k + PD < 11) bf[k + PD] = act_frag(sa, sb, (k + PD) * 1024);
        SB0();
        acc[i][1] = mfma32(w2[k][1], bf[k], acc[i][1]);
        SB0();
      }
      if (lo < 3) { acc[i][0] = mfma32(w2[10][0], bf[10], acc[i][0]); acc[i][1] = mfma32(w2[10][1], bf[10], acc[i][1]); }
      SB0();
    };
    const u32 pw = lo * PQ + t * 272 + hi * 128 + G * 16;
    auto dump = [&](int i, u32 buf) { *(LAS f32x4*)(buf + pw) = acc[i][0]; *(LAS f32x4*)(buf + pw + 64) = acc[i][1]; };
    const u32 pr = (2 * w + (lane >> 5)) * 272 + (lane & 31) * 8;
    char* const op = p.out + (size_t)(T0 + 2 * w) * 1024 + j * 128;
    const u32 ov = (lane >> 5) * 1024 + (lane & 31) * 4;
    auto fin = [&](int i, u32 buf) {
      const f32x2 sv = (ldsf2(buf + pr) + ldsf2(buf + pr + PQ)) + (ldsf2(buf + pr + 2 * PQ) + ldsf2(buf + pr + 3 * PQ));
      *(u32*)(op + i * 16384 + ov) = pk2(sv[0], sv[1]);
    };
    const u32 fl23 = G_FLC + (2 + hi) * FBL + lo * 8 * FSP;
    bool got = (gflag_poll(gw, fl23, tag, lane, 8) & 0xff) == 0xff;
    VMWAIT0();
    BARRIER();
    STAMP(12);
    if (got && hi == 0) { fhalf(2, 0, 0); fhalf(2, 0, 1); }
    const u64 pv_ = __hip_atomic_load((GAS u64*)(gw + fl23 + (lane & 7) * FSP), __ATOMIC_RELAXED, __HIP_MEMORY_SCOPE_AGENT);
    tile(0, L_F1);
    dump(0, L_P4);
    const bool got2 = got || (__builtin_amdgcn_ballot_w64(pv_ == tag) & 0xff) == 0xff;
    MEMFENCE();
    BARRIER();
    if (got2 && !(got && hi == 0)) { fhalf(2 + hi, hi ? L_F1 : 0, 0); fhalf(2 + hi, hi ? L_F1 : 0, 1); }
    fin(0, L_P4);
    tile(1, L_F2);
    if (!got2) { poll8(gw, fl23, tag, lane); fhalf(2 + hi, hi ? L_F1 : 0, 0); fhalf(2 + hi, hi ? L_F1 : 0, 1); }
    STAMP(15);
    VMWAIT0();
    BARRIER();
    STAMP(13);
    dump(1, L_P4);
    tile(2, 0);
    dump(2, L_F2);
    tile(3, L_F1);
    dump(3, L_F2 + 20480);
    BARRIER();
    fin(1, L_P4); fin(2, L_F2); fin(3, L_F2 + 20480);
  }
  if (w == 0 && j == 0 && lane == 0) __hip_atomic_store((GAS u64*)(gw + G_EPOCH), tag, __ATOMIC_RELAXED, __HIP_MEMORY_SCOPE_AGENT);
  STAMP(8); CSTAMP(11);
  STAMP_OUT();
}
}

extern "C" void kernel_launch(void* const* d_in, const int* in_sizes, int n_in, void* d_out, int out_size, void* d_ws, size_t ws_size,
                              hipStream_t stream) {
  fb::Params p;
  p.x = (const char*)d_in[0]; p.wqkv = (const char*)d_in[1]; p.wo = (const char*)d_in[2];
  p.w1 = (const char*)d_in[4]; p.w2 = (const char*)d_in[5];
  p.out = (char*)d_out; p.ws = (char*)d_ws;
  (void)hipFuncSetAttribute((const void*)fb::fused_block, hipFuncAttributeMaxDynamicSharedMemorySize, fb::L_TOTAL);
  fb::fused_block<<<256, 512, fb::L_TOTAL, stream>>>(p);
}
```

```cpp
#include <hip/hip_runtime.h>
#include <hip/hip_bf16.h>
#include <utility>
#pragma clang diagnostic ignored "-Wint-to-pointer-cast"
#pragma clang diagnostic ignored "-Wc++20-extensions"
#define MAPC 1
#define TOUCH 3
#define TW 2
#define TSTEP 65536u
#define TO 5
#define FLW 1
#define APRIO0 0
#define OUTSC 0
#define P1ONLY 0
#define P4S(i)
namespace fb {
typedef unsigned u32; typedef unsigned long long u64; typedef unsigned short u16;
typedef u32 v4u __attribute__((ext_vector_type(4)));
typedef u32 v2u __attribute__((ext_vector_type(2)));
typedef float f32x4 __attribute__((ext_vector_type(4)));
typedef float f32x2 __attribute__((ext_vector_type(2)));
typedef __bf16 bf8 __attribute__((ext_vector_type(8)));
typedef __bf16 bf2 __attribute__((ext_vector_type(2)));
typedef short s4 __attribute__((ext_vector_type(4)));
#define LAS __attribute__((address_space(3)))
#define GAS __attribute__((address_space(1)))
#define DEV __device__ __forceinline__
#define IC(x) std::integral_constant<int, (x)>{}
#define UNROLL_SEQ(fn, N) [&]<int... Is_>(std::integer_sequence<int, Is_...>) { (fn(std::integral_constant<int, Is_>{}), ...); }(std::make_integer_sequence<int, N>{})

constexpr u32 G_EPOCH = 0;
constexpr u32 G_O = 8192;
constexpr u32 G_H = G_O + 65536;
constexpr u32 G_F = G_H + 65536;
constexpr u32 F_TAU = 86 * 512;
constexpr u32 G_ST = 315392 + 1024;
constexpr u32 FSP = 256, FBL = 32 * FSP;
constexpr u32 G_FLA = 327680, G_FLB = G_FLA + 2 * FBL, G_FLC = G_FLB + 2 * FBL, G_BYTES = G_FLC + 4 * FBL;
static_assert(4 * G_BYTES <= (2u << 20) && G_ST + 8192 <= G_FLA, "workspace map");
__host__ __device__ constexpr size_t group_base(int g) { return (size_t)(g >> 2) * (2u << 20) + (size_t)(g & 3) * G_BYTES; }
constexpr u32 WS_DBG = 16u << 20;
constexpr u32 L_X = 0, L_RING = 81920, SLOT = 12288, L_CTRL = 155648, L_TOTAL = 163840;
constexpr u32 L_W2 = 65536, L_P3 = 114688, C_S2 = 147456;
constexpr u32 FPART = 11264, L_F1 = 65536, L_F2 = 110592, L_P4 = 45056, PQ = 4352;
constexpr u32 C_R = L_CTRL;
constexpr u32 C_CS1 = L_CTRL + 1024;
constexpr u32 C_FLAGS = L_CTRL + 768, FL_W1L = 0, FL_W1C = 4, C_TAG = L_CTRL + 640, NSLOT = 6;
constexpr u32 SPIN_G = 1u << 16, SPIN_L = 1u << 20;

struct Params { const char* x; const char* wqkv; const char* wo; const char* w1; const char* w2; char* out; char* ws; };

DEV f32x4 mfma32(v4u a, v4u b, f32x4 c) { return __builtin_amdgcn_mfma_f32_16x16x32_bf16(__builtin_bit_cast(bf8, a), __builtin_bit_cast(bf8, b), c, 0, 0, 0); }
DEV f32x4 mfma16(v2u a, v2u b, f32x4 c) { return __builtin_amdgcn_mfma_f32_16x16x16bf16_1k(__builtin_bit_cast(s4, a), __builtin_bit_cast(s4, b), c, 0, 0, 0); }
DEV void mfma32t(f32x4& c, v4u a, v4u b) { asm volatile("v_mfma_f32_16x16x32_bf16 %0, %1, %2, %0" : "+v"(c) : "v"(a), "v"(b)); }
DEV void mfma32z(f32x4& c, v4u a, v4u b) { asm volatile("v_mfma_f32_16x16x32_bf16 %0, %1, %2, 0" : "=&v"(c) : "v"(a), "v"(b)); }
DEV u32 pk2(float a, float b) { bf2 p = {(__bf16)a, (__bf16)b}; return __builtin_bit_cast(u32, p); }
DEV v2u pk4(f32x4 v) { return v2u{pk2(v[0], v[1]), pk2(v[2], v[3])}; }
DEV float asf(u32 u) { return __builtin_bit_cast(float, u); }
DEV u32 asu(float f) { return __builtin_bit_cast(u32, f); }
DEV f32x4 unpk4(u32 lo, u32 hi) { return f32x4{asf(lo << 16), asf(lo & 0xffff0000u), asf(hi << 16), asf(hi & 0xffff0000u)}; }
DEV u32 uni(u32 v) { return __builtin_amdgcn_readfirstlane(v); }
DEV v4u lds128(u32 a) { return *(const LAS v4u*)a; }
DEV v2u lds64(u32 a) { return *(const LAS v2u*)a; }
DEV f32x4 ldsf4(u32 a) { return *(const LAS f32x4*)a; }
DEV f32x2 ldsf2(u32 a) { return *(const LAS f32x2*)a; }
DEV float ldsf(u32 a) { return *(const LAS float*)a; }
DEV v2u ldstr(u32 a) { return __builtin_bit_cast(v2u, __builtin_amdgcn_ds_read_tr16_b64_v4i16((LAS s4*)a)); }
DEV float vmax(float a, float b) { float r; asm("v_max_f32 %0, %1, %2" : "=v"(r) : "v"(a), "v"(b)); return r; }
#define MEMFENCE() asm volatile("" ::: "memory")
#define SB0() __builtin_amdgcn_sched_barrier(0)
#define BARRIER() do { SB0(); asm volatile("s_waitcnt lgkmcnt(0)\n\ts_barrier" ::: "memory"); SB0(); } while (0)
#define SGB(m, n) __builtin_amdgcn_sched_group_barrier(m, n, 0)
#define VMWAIT0() asm volatile("s_waitcnt vmcnt(0)" ::: "memory")
#define LGWAIT0() asm volatile("s_waitcnt lgkmcnt(0)" ::: "memory")
template <int N> DEV void vmwait() { asm volatile("s_waitcnt vmcnt(%0)" ::"i"(N) : "memory"); }
#define GLD "global_load_lds_dwordx4 "
DEV void dma3(const char* sb, u32 v, u32 dst) {
  asm volatile("s_mov_b32 m0, %2\n\ts_nop 0\n\t" GLD "%0, %1\n\t" GLD "%0, %1 offset:1024\n\t" GLD "%0, %1 offset:2048" ::"v"(v), "s"(sb), "s"(dst) : "memory");
}
DEV void dma2v(const char* sb, u32 v0, u32 v1, u32 dst) {
  asm volatile("s_mov_b32 m0, %3\n\ts_nop 0\n\t" GLD "%0, %2\n\t" GLD "%1, %2 offset:1024" ::"v"(v0), "v"(v1), "s"(sb), "s"(dst) : "memory");
}
DEV void dma3v(const char* sb, u32 v0, u32 v1, u32 v2, u32 dst) {
  asm volatile("s_mov_b32 m0, %4\n\ts_nop 0\n\t" GLD "%0, %3\n\t" GLD "%1, %3 offset:1024\n\t" GLD "%2, %3 offset:2048"
               ::"v"(v0), "v"(v1), "v"(v2), "s"(sb), "s"(dst) : "memory");
}
DEV void dma4v(const char* sb, u32 v0, u32 v1, u32 v2, u32 v3, u32 dst) {
  asm volatile("s_mov_b32 m0, %5\n\ts_nop 0\n\t" GLD "%0, %4\n\t" GLD "%1, %4 offset:1024\n\t" GLD "%2, %4 offset:2048\n\t" GLD "%3, %4 offset:3072"
               ::"v"(v0), "v"(v1), "v"(v2), "v"(v3), "s"(sb), "s"(dst) : "memory");
}
DEV void dma4w(const char* sb, u32 v0, u32 v1, u32 v2, u32 v3, u32 dst) {
  asm volatile("s_mov_b32 m0, %5\n\ts_nop 0\n\t" GLD "%0, %4\n\t" GLD "%1, %4 offset:1152\n\t" GLD "%2, %4 offset:2048\n\t" GLD "%3, %4 offset:3200"
               ::"v"(v0), "v"(v1), "v"(v2), "v"(v3), "s"(sb), "s"(dst) : "memory");
}
DEV void dma_more2048(const char* sb, u32 v) { asm volatile(GLD "%0, %1 offset:2048" ::"v"(v), "s"(sb) : "memory"); }
DEV void touch(const char* sb, u32 v) {
  asm volatile("s_mov_b32 m0, %2\n\ts_nop 0\n\tglobal_load_lds_dword %0, %1" ::"v"(v), "s"(sb), "s"(L_CTRL + 3072) : "memory");
}
DEV void dma1_sc1(const char* sb, u32 v, u32 dst) {
  asm volatile("s_mov_b32 m0, %2\n\ts_nop 0\n\t" GLD "%0, %1 sc1" ::"v"(v), "s"(sb), "s"(dst) : "memory");
}
DEV void dma4_sc1(const char* sb, u32 v, u32 dst) {
  asm volatile("s_mov_b32 m0, %2\n\ts_nop 0\n\t" GLD "%0, %1 sc1\n\t" GLD "%0, %1 offset:1024 sc1\n\t" GLD "%0, %1 offset:2048 sc1\n\t"
               GLD "%0, %1 offset:3072 sc1" ::"v"(v), "s"(sb), "s"(dst) : "memory");
}
DEV void dma8_sc1(const char* sb, u32 v, u32 dst) {
  asm volatile("s_mov_b32 m0, %2\n\ts_nop 0\n\t" GLD "%0, %1 offset:-4096 sc1\n\t" GLD "%0, %1 offset:-3072 sc1\n\t" GLD "%0, %1 offset:-2048 sc1\n\t"
               GLD "%0, %1 offset:-1024 sc1\n\t" GLD "%0, %1 sc1\n\t" GLD "%0, %1 offset:1024 sc1\n\t" GLD "%0, %1 offset:2048 sc1\n\t"
               GLD "%0, %1 offset:3072 sc1" ::"v"(v), "s"(sb), "s"(dst) : "memory");
}
DEV void dma2_sc1(const char* sb, u32 v, u32 dst) {
  asm volatile("s_mov_b32 m0, %2\n\ts_nop 0\n\t" GLD "%0, %1 sc1\n\t" GLD "%0, %1 offset:1024 sc1" ::"v"(v), "s"(sb), "s"(dst) : "memory");
}
DEV void dma_more2048_sc1(const char* sb, u32 v) { asm volatile(GLD "%0, %1 offset:2048 sc1" ::"v"(v), "s"(sb) : "memory"); }
DEV void lflag_set(u32 word, u32 v) {
  MEMFENCE();
  __hip_atomic_store((LAS u32*)(C_FLAGS + word * 4), v, __ATOMIC_RELAXED, __HIP_MEMORY_SCOPE_WORKGROUP);
  MEMFENCE();
}
DEV u32 lflag_min4(u32 word) {
  v4u v;
  asm volatile("ds_read_b128 %0, %1\n\ts_waitcnt lgkmcnt(0)" : "=v"(v) : "v"(C_FLAGS + word * 4) : "memory");
  u32 m = min(min(v.x, v.y), min(v.z, v.w));
  return uni(m);
}
DEV u32 lflag_get(u32 word) {
  u32 v;
  asm volatile("ds_read_b32 %0, %1\n\ts_waitcnt lgkmcnt(0)" : "=v"(v) : "v"(C_FLAGS + word * 4) : "memory");
  return uni(v);
}
DEV void lwait4(u32 word, u32 want) {
#pragma unroll 1
  for (u32 s = 0; s < SPIN_L && lflag_min4(word) < want; ++s) {} }
DEV void lwait1(u32 word, u32 want) {
#pragma unroll 1
  for (u32 s = 0; s < SPIN_L && lflag_get(word) < want; ++s) {} }
DEV void gflag_pub(char* gw, u32 off, u64 tag, int lane) {
  VMWAIT0();
  if (lane < FLW) __hip_atomic_store((GAS u64*)(gw + off + lane * 8), tag, __ATOMIC_RELAXED, __HIP_MEMORY_SCOPE_AGENT);
  MEMFENCE();
}
DEV u64 gflag_poll(char* gw, u32 off, u64 tag, int lane, int n) {
  u64 v = __hip_atomic_load((GAS u64*)(gw + off + (lane & 7) * FSP + ((lane & (n - 1)) >> 3) * (32 * FSP)), __ATOMIC_RELAXED, __HIP_MEMORY_SCOPE_AGENT);
  u64 m = __builtin_amdgcn_ballot_w64(v == tag);
  MEMFENCE();
  return m;
}
DEV bool vm_idle() { return (__builtin_amdgcn_s_getreg((31 << 11) | 7) & 0xC0000Fu) == 0; }
DEV u64 mix64(u64 z) {
  z += 0x9E3779B97F4A7C15ull; z = (z ^ (z >> 30)) * 0xBF58476D1CE4E5B9ull; z = (z ^ (z >> 27)) * 0x94D049BB133111EBull; return z ^ (z >> 31);
}
DEV float xg_sum(float v) {
  auto a = __builtin_amdgcn_permlane16_swap(asu(v), asu(v), false, false);
  v = asf(a[0]) + asf(a[1]);
  auto b = __builtin_amdgcn_permlane32_swap(asu(v), asu(v), false, false);
  return asf(b[0]) + asf(b[1]);
}
template <int CTRL> DEV float dppf(float v) { return asf(__builtin_amdgcn_update_dpp(0u, asu(v), CTRL, 0xf, 0xf, false)); }
DEV void ln_coef(float s, float q, float& r, float& b) {
  float m = s * (1.0f / 512.0f);
  float var = fmaxf(q * (1.0f / 512.0f) - m * m, 0.0f);
  r = __builtin_amdgcn_rsqf(var + 1e-5f);
  b = -m * r;
}
#define STAMP(i)
#define CSTAMP(i)
#define STAMP_DECL
#define STAMP_OUT()

struct Frag4 { v4u f[4]; };
DEV void wcell_dma_half(const char* src, u32 ve, u32 cell, int h) { dma2v(src + h * 8192, ve, (ve ^ 64) + 3072, cell + h * 2048); }
DEV u32 wcell_voff(int lane, int j) {
  const int r = lane >> 3;
  return (16 * (r >> 2) + (r & 3)) * 1024 + j * 128 + ((((lane & 7) >> 1) ^ ((r >> 1) & 1)) * 32) + (lane & 1) * 16;
}
DEV u32 wcell_roff(int lane) {
  const int G = lane >> 4, q = (lane & 15) >> 2;
  return G * 1024 + q * 128 + ((((q >> 1) & 1) | ((G & 1) << 1)) * 32) + (lane & 3) * 8;
}
DEV v4u trfrag(u32 ad, u32 d2) { v2u lo = ldstr(ad), hi = ldstr(ad + d2); return v4u{lo.x, lo.y, hi.x, hi.y}; }
DEV v4u act_frag(u32 a, u32 a2, u32 off) { v2u lo = lds64(a + off), hi = lds64(a2 + off); return v4u{lo.x, lo.y, hi.x, hi.y}; }
DEV void dma1(const char* sb, u32 v, u32 dst) { asm volatile("s_mov_b32 m0, %2\n\ts_nop 0\n\t" GLD "%0, %1" ::"v"(v), "s"(sb), "s"(dst) : "memory"); }
DEV void dma5_sc1(const char* sb, u32 v, u32 dst) {
  asm volatile("s_mov_b32 m0, %2\n\ts_nop 0\n\t" GLD "%0, %1 offset:-1024 sc1\n\t" GLD "%0, %1 sc1\n\t" GLD "%0, %1 offset:1024 sc1\n\t"
               GLD "%0, %1 offset:2048 sc1\n\t" GLD "%0, %1 offset:3072 sc1" ::"v"(v), "s"(sb), "s"(dst) : "memory");
}
DEV void dma_morem2048_sc1(const char* sb, u32 v) { asm volatile(GLD "%0, %1 offset:-2048 sc1" ::"v"(v), "s"(sb) : "memory"); }
DEV void touch64(const char* ad) { asm volatile("s_mov_b32 m0, %1\n\ts_nop 0\n\tglobal_load_lds_dword %0, off sc1" ::"v"(ad), "s"(L_CTRL + 3072) : "memory"); }
DEV void poll_issue(u64& v, const char* sb, u32 off) { asm volatile("global_load_dwordx2 %0, %1, %2 sc1" : "=v"(v) : "v"(off), "s"(sb) : "memory"); }
template <int N> DEV void poll_wait(u64& v) { asm volatile("s_waitcnt vmcnt(%1)" : "+v"(v) : "i"(N) : "memory"); }
DEV void poll8(char* gw, u32 off, u64 tag, int lane) {
#pragma unroll 1
  for (u32 s = 0; s < SPIN_G && (gflag_poll(gw, off, tag, lane, 8) & 0xff) != 0xff; ++s) {}
}

constexpr int SCH[17][4] = {{1, 2, 3, 4},     {5, 18, -1, -1},  {6, 19, -1, -1},  {7, 20, -1, -1},  {8, 21, -1, -1},  {9, 22, -1, -1},
                            {10, 23, -1, -1}, {11, -1, -1, -1}, {12, -1, -1, -1}, {13, -1, -1, -1}, {14, -1, -1, -1}, {15, -1, -1, -1},
                            {24, -1, -1, -1}, {25, -1, -1, -1}, {26, -1, -1, -1}, {27, -1, -1, -1}, {30, 31, -1, -1}};
constexpr int item_ops(int it) { return it < 0 ? 0 : it < 16 ? 3 : it < 24 ? ((it & 1) ? 2 : 3) : it < 30 ? 4 : 3; }
constexpr int item_end(int it) { int n = 0; for (int gI = 0; gI < 17; ++gI) for (int k = 0; k < 4; ++k) { n += item_ops(SCH[gI][k]); if (SCH[gI][k] == it) return n; } return 0; }
constexpr int issued_before_barrier(int b) { int n = 0; for (int gI = 0; gI <= b; ++gI) for (int k = 0; k < 4; ++k) n += item_ops(SCH[gI][k]); return n; }
constexpr int xpair_at(int b) { return b < 4 ? 0 : b < 7 ? 1 : b < 9 ? 2 : 3; }
constexpr int need_before_barrier(int b) { return item_end(b) > item_end(17 + 2 * xpair_at(b)) ? item_end(b) : item_end(17 + 2 * xpair_at(b)); }

__global__ void __launch_bounds__(512, 2) fused_block(Params p) {
  extern __shared__ __attribute__((aligned(16))) char smem[];
  const int tid = threadIdx.x, lane = tid & 63;
  const int w = uni(tid >> 6), lo = w & 3, hi = w >> 2;
  const int bid = blockIdx.x, xcd = bid & 7, idx = bid >> 3;
  const int g = xcd * 4 + (idx >> 3), j = idx & 7;
  const int T0 = g * 64;
  const int t = lane & 15, G = lane >> 4, rho = lane >> 3;
  char* const gw = p.ws + group_base(g);
  const __amdgpu_buffer_rsrc_t rws = __builtin_amdgcn_make_buffer_rsrc((void*)gw, 0, G_BYTES, 0x00020000);
  STAMP_DECL;
  STAMP(0); CSTAMP(10);
  asm volatile("" ::"s"(smem));
  const u32 wv = (8 * lo + rho) * 3072 + j * 128 + (((((lane & 7) >> 1) ^ (((rho >> 1) & 1) | ((lo & 1) << 1))) * 32) + (lane & 1) * 16);
  auto wload = [&](int c) { dma3(p.wqkv + (size_t)((c + 2 * xcd) & 15) * (32 * 3072), wv, L_RING + (c % NSLOT) * SLOT + lo * 3072); };
  u32 zz = 0; asm volatile("" : "+v"(zz));
  u64 epv;
  asm volatile("global_load_dwordx2 %0, %1, %2 sc1" : "=v"(epv) : "v"(zz), "s"(gw + G_EPOCH) : "memory");
  if (hi == 0) wload(0); else { wload(1); wload(2); wload(3); wload(4); }
  u32 xv[5];
#pragma unroll
  for (int n = 0; n < 5; ++n) {
    const int i = (5 * lo + n) % 10, row = T0 - 16 + 8 * i + rho;
    xv[n] = max(row, 0) * 1024 + ((((lane & 7) ^ (rho >> 1)) << 4) ^ ((i & 1) * 64)) + 2048 - (n < 3 ? n : n - 3) * 1024;
  }
  auto xload = [&](int P, int part) {
    const char* src = p.x - 2048 + ((2 * P + (lo >> 1) + xcd) & 7) * 128;
    if (part == 0) dma3v(src, xv[0], xv[1], xv[2], L_X + P * 20480 + 5 * lo * 1024);
    else dma2v(src, xv[3], xv[4], L_X + P * 20480 + (5 * lo + 3) * 1024);
  };
  if (hi == 0) { xload(0, 0); xload(0, 1); }
  const v4u ones = {0x3F803F80u, 0x3F803F80u, 0x3F803F80u, 0x3F803F80u};
  const f32x4 zero4 = {0, 0, 0, 0};
  const int Tj = 22 * (j >> 1) + ((j & 1) ? (j < 6 ? 12 : 10) : 0);
  const bool full = !(j & 1) && j < 6;
  const int lt = (full || lo < 3) ? 3 * lo : 8;
  const int nv = (full || lo < 2) ? 3 : 2;
  const u32 wev = wcell_voff(lane, j), ro = wcell_roff(lane);
  u32 w1v[3], ra[3];
  {
    const int r8 = (lane >> 2) & 7, mis = (r8 + (Tj >> 1)) & 1;
#pragma unroll
    for (int n = 0; n < 3; ++n) {
      int q6 = (2 * n + (lane >> 5) + 6 - mis) % 6;
      if (!full && q6 == 5) q6 = mis ? 0 : 4;
      w1v[n] = (16 * (r8 >> 2) + (r8 & 3) + 4 * lo) * 2752 + (Tj + 2 * q6 + (((lane >> 1) ^ lo) & 1)) * 32 + (lane & 1) * 16 + 2048 - n * 1024;
      ra[n] = L_RING + G * 3072 + ((((lt + n) >> 1) + mis) % 6) * 512 + ((lane & 15) >> 2) * 64 + ((((lt + n) ^ G) & 1) * 32) + (lane & 3) * 8;
      asm volatile("" : "+v"(ra[n]));
    }
  }
  auto w1load = [&](int ks, int sl) { dma3v(p.w1 - 2048 + (size_t)(32 * ks) * 2752, w1v[0], w1v[1], w1v[2], L_RING + sl * SLOT + lo * 3072); };
  v4u w1[8][3];
  auto w1read = [&](int r, int sl) {
#pragma unroll
    for (int u = 0; u < 3; ++u) w1[r][u] = trfrag(ra[u] + sl * SLOT, 256);
  };
  const char* w2s = p.w2 + (size_t)(11 * lo) * 32768;
  Frag4 wo[4];
  auto woread = [&]() {
#pragma unroll
    for (int s = 0; s < 4; ++s)
#pragma unroll
      for (int c = 0; c < 4; ++c) wo[s].f[c] = trfrag(s * 16384 + lo * 4096 + (ro ^ (c * 32)), 512);
  };
  v2u xres[2];
  const u32 xra = L_X + (((j - xcd) & 7) * 5 + lo + 1) * 2048 + (t >> 3) * 1024 + (t & 7) * 128 + (((4 * hi + (G >> 1)) ^ ((t >> 1) & 7)) << 4) + (G & 1) * 8;
  const u32 xl = (t >> 3) * 1024 + (t & 7) * 128 + ((G ^ ((t >> 1) & 7)) << 4);
  u64 tag;
  int hr_ = hi;
  asm volatile("" : "+s"(hr_));

  if (hr_ == 0) {
    __builtin_amdgcn_s_setprio(2);
    if (w == 0 && lane < 8) *(LAS u32*)(C_FLAGS + lane * 4) = 0;
    f32x4 aq[5], ak[5], av[5];
#pragma unroll
    for (int i = 0; i < 5; ++i) { aq[i] = zero4; ak[i] = zero4; av[i] = zero4; asm volatile("" : "+v"(aq[i]), "+v"(ak[i]), "+v"(av[i])); }
    const int q = (lane & 15) >> 2;
    const u32 woff = L_RING + G * 3072 + q * 128 + ((lo ^ (((q >> 1) & 1) | ((G & 1) << 1))) * 32) + (lane & 3) * 8;
    struct Fr { v4u w[3]; v4u x[5]; };
    auto ldfr = [&](Fr& f, u32 wa, u32 xa) {
#pragma unroll
      for (int m = 0; m < 3; ++m) f.w[m] = trfrag(wa + m * 1024, 512);
#pragma unroll
      for (int i = 0; i < 5; ++i) f.x[i] = lds128(xa + i * 2048);
    };
    auto mma = [&](Fr& f) {
      v4u x0 = f.x[0];
      if (t == 0) x0 = ones;
#pragma unroll
      for (int i = 1; i < 5; ++i) { aq[i] = mfma32(f.w[0], f.x[i], aq[i]); ak[i] = mfma32(f.w[1], f.x[i], ak[i]); av[i] = mfma32(f.x[i], f.w[2], av[i]); }
      aq[0] = mfma32(f.w[0], x0, aq[0]); ak[0] = mfma32(f.w[1], x0, ak[0]); av[0] = mfma32(x0, f.w[2], av[0]);
    };
    auto pat = [&]() {
#pragma unroll
      for (int i = 0; i < 6; ++i) { SGB(0x8, 1); SGB(0x100, 2); }
      SGB(0x8, 9);
    };
    Fr f0, f1;
    u32 so = 0, xa = L_X + xl;
    VMWAIT0();
    BARRIER();
    STAMP(1); CSTAMP(24);
    ldfr(f0, woff, xa);
    asm volatile("" : "+v"(epv));
    tag = mix64(((u64)uni((u32)(epv >> 32)) << 32) | uni((u32)epv));
    SB0();
#pragma unroll 1
    for (int it = 0; it < 7; ++it) {
      so = (so == (NSLOT - 1) * SLOT) ? 0 : so + SLOT;
      BARRIER();
      ldfr(f1, woff + so, xa ^ 64);
      mma(f0);
      pat();
      so = (so == (NSLOT - 1) * SLOT) ? 0 : so + SLOT;
      xa += 10240;
      BARRIER();
      ldfr(f0, woff + so, xa);
      mma(f1);
      pat();
      SB0();
      if (it <= 4) { xres[0] = lds64(xra); xres[1] = lds64(xra ^ 32); }
      if (it == TW) {
        const u32 id = bid * 102 + min(lo * 26 + (lane < 26 ? lane : 25), 101);
        touch64(id < 4096 ? p.wo + id * 128 : id < 15104 ? p.w1 + (id - 4096) * 128 : p.w2 + (id - 15104) * 128);
      }
      if (it == TO) {
        if (lane < 8) touch64(gw + G_FLA + (lane * 32 + j * 4 + lo) * FSP);
      }
    }
    BARRIER();
    ldfr(f1, woff + 3 * SLOT, xa ^ 64);
    f32x2 rb1[5]; f32x4 rbA[5], rbB[5];
    {
      u32 ca = C_R + t * 8, cb = C_R + G * 32;
      asm volatile("" : "+v"(ca), "+v"(cb));
#pragma unroll
      for (int i = 0; i < 5; ++i) { rb1[i] = ldsf2(ca + i * 128); rbA[i] = ldsf4(cb + i * 128); rbB[i] = ldsf4(cb + i * 128 + 16); }
    }
    mma(f0);
#pragma unroll
    for (int i = 0; i < 13; ++i) { SGB(0x8, 1); SGB(0x100, 2); }
    SGB(0x8, 2);
    BARRIER();
    {
      v4u x0 = f1.x[0];
      if (t == 0) x0 = ones;
      aq[0] = mfma32(f1.w[0], x0, aq[0]); ak[0] = mfma32(f1.w[1], x0, ak[0]); av[0] = mfma32(x0, f1.w[2], av[0]);
#pragma unroll
      for (int i = 1; i < 5; ++i) { ak[i] = mfma32(f1.w[1], f1.x[i], ak[i]); aq[i] = mfma32(f1.w[0], f1.x[i], aq[i]); }
#pragma unroll
      for (int i = 1; i < 5; ++i) av[i] = mfma32(f1.x[i], f1.w[2], av[i]);
    }
    STAMP(2); CSTAMP(25);
    v2u kb[5], vb[5], qb[5];
    f32x4 csq, csk;
#pragma unroll
    for (int r = 0; r < 4; ++r) { csq[r] = dppf<0x150>(aq[0][r]); csk[r] = dppf<0x150>(ak[0][r]); }
    const float csv = __shfl(av[0][0], t);
#pragma unroll
    for (int i = 0; i < 5; ++i) {
      kb[i] = pk4(ak[i] * rb1[i][0] + csk * rb1[i][1]);
      if (i > 0) qb[i] = pk4(aq[i] * rb1[i][0] + csq * rb1[i][1]);
    }
    SB0();
    f32x4 sp[4], sc[4];
#pragma unroll
    for (int i = 0; i < 4; ++i) { sp[i] = mfma16(kb[i], qb[i + 1], zero4); sc[i] = mfma16(kb[i + 1], qb[i + 1], zero4); }
#pragma unroll
    for (int i = 0; i < 5; ++i)
      vb[i] = v2u{pk2(av[i][0] * rbA[i][0] + csv * rbA[i][1], av[i][1] * rbA[i][2] + csv * rbA[i][3]),
                  pk2(av[i][2] * rbB[i][0] + csv * rbB[i][1], av[i][3] * rbB[i][2] + csv * rbB[i][3])};
    SB0();
    const bool par = t & 1;
    const bool cur0 = 4 * G + (t & 1) <= t, cur1 = 4 * G + (t & 1) + 2 <= t;
    const u32 sh = (t & 1) * 16;
    float z0[4], z1[4], m[4];
#pragma unroll
    for (int i = 0; i < 4; ++i) {
      const float c0 = par ? sc[i][1] : sc[i][0], c1 = par ? sc[i][3] : sc[i][2];
      float p0 = par ? sp[i][1] : sp[i][0], p1 = par ? sp[i][3] : sp[i][2];
      if (i == 0 && g == 0) { p0 = -4e9f; p1 = -4e9f; }
      z0[i] = cur0 ? c0 : p0; z1[i] = cur1 ? c1 : p1;
      m[i] = vmax(z0[i], z1[i]);
    }
    SB0();
#pragma unroll
    for (int i = 0; i < 4; ++i) { auto s_ = __builtin_amdgcn_permlane16_swap(asu(m[i]), asu(m[i]), false, false); m[i] = vmax(asf(s_[0]), asf(s_[1])); }
    SB0();
#pragma unroll
    for (int i = 0; i < 4; ++i) { auto s_ = __builtin_amdgcn_permlane32_swap(asu(m[i]), asu(m[i]), false, false); m[i] = vmax(asf(s_[0]), asf(s_[1])); }
    SB0();
    f32x4 o[4], den[4];
#pragma unroll
    for (int i = 0; i < 4; ++i) {
      const float e0 = __builtin_amdgcn_exp2f((z0[i] - m[i]) * 0.36067376022f), e1 = __builtin_amdgcn_exp2f((z1[i] - m[i]) * 0.36067376022f);
      const u32 u = pk2(e0, e1);
      const u32 w0 = (u & 0xffffu) << sh, w1_ = (u >> 16) << sh;
      const v4u P = {cur0 ? 0u : w0, cur1 ? 0u : w1_, cur0 ? w0 : 0u, cur1 ? w1_ : 0u};
      const v4u V = {vb[i].x, vb[i].y, vb[i + 1].x, vb[i + 1].y};
      den[i] = mfma32(ones, P, zero4);
      o[i] = mfma32(V, P, zero4);
    }
    SB0();
#pragma unroll
    for (int i = 0; i < 4; ++i) {
      const v2u ob = pk4(o[i] * __builtin_amdgcn_rcpf(den[i][0]));
      __builtin_amdgcn_raw_buffer_store_b64(ob, rws, lane * 8, G_O + (i * 32 + 4 * j + lo) * 512, 16);
    }
    SB0();
    STAMP(23);
    woread();
    gflag_pub(gw, G_FLA + (j * 4 + lo) * FSP, tag, lane);
    STAMP(3);
    __builtin_amdgcn_s_setprio(0);
#pragma unroll
    for (int r = 0; r < 6; ++r)
#pragma unroll
      for (int u = 0; u < 3; ++u) w1[r][u] = v4u{0, 0, 0, 0};
  } else {
    __builtin_amdgcn_s_setprio(3);
    auto item = [&](auto I) {
      constexpr int it = decltype(I)::value;
      if constexpr (it >= 30) w1load(it - 30, (it - 30 + 4) % 6);
      else if constexpr (it >= 24) { wcell_dma_half(p.wo + (size_t)(4 * lo + it - 24) * 32768, wev, (it - 24) * 16384 + lo * 4096, 0); wcell_dma_half(p.wo + (size_t)(4 * lo + it - 24) * 32768, wev, (it - 24) * 16384 + lo * 4096, 1); }
      else if constexpr (it >= 16) xload((it - 16) / 2, it & 1);
      else if constexpr (it >= 0) wload(it);
    };
    const u32 xo0 = L_X + lo * 2048 + xl;
    const int t4 = 4 * lo + (t & 3);
    const u32 x4 = L_X + ((t >> 3) * 5 + 4) * 2048 + (t4 >> 3) * 1024 + (t4 & 7) * 128 + ((((t >> 2) & 1) * 4 + G) << 4);
    f32x4 s0 = zero4, q0 = zero4, s4 = zero4, q4 = zero4;
    v4u xs[2][3];
    float sq4 = 0.f;
    auto diag = [&](f32x4 v) { const float l_ = (t & 1) ? v[1] : v[0], h_ = (t & 1) ? v[3] : v[2]; return (t & 2) ? h_ : l_; };
    auto step = [&](auto I) {
      constexpr int be = decltype(I)::value;
      vmwait<issued_before_barrier(be) - need_before_barrier(be)>();
      if (P1ONLY) STAMP(12 + be);
      BARRIER();
      if constexpr (be >= 4 && be <= 10) {
#pragma unroll
        for (int k = 0; k < 3; ++k) {
          const int i = 3 * (be - 4) + k, P = i / 5, kk = i % 5;
          if (i < 20) xs[be & 1][k] = kk < 4 ? lds128((xo0 ^ ((kk & 1) * 64)) + (2 * P + (kk >> 1)) * 10240) : lds128(x4 + P * 20480);
        }
      }
      if constexpr (be == 10) { xres[0] = lds64(xra); xres[1] = lds64(xra ^ 32); }
      SB0();
      item(IC(SCH[be + 1][0])); item(IC(SCH[be + 1][1]));
      SB0();
      if constexpr (be >= 5 && be <= 11) {
#pragma unroll
        for (int k = 0; k < 3; ++k) {
          constexpr int i0 = 3 * (be - 5);
          if (i0 + k < 20) {
            const v4u xx = xs[(be - 1) & 1][k];
            if ((i0 + k) % 5 < 4) { s0 = mfma32(ones, xx, s0); q0 = mfma32(xx, xx, q0); }
            else { s4 = mfma32(ones, xx, s4); q4 = mfma32(xx, xx, q4); }
          }
        }
      }
      if constexpr (be == 2) { asm volatile("" : "+v"(epv)); tag = mix64(((u64)uni((u32)(epv >> 32)) << 32) | uni((u32)epv)); }
      if constexpr (be == 12) {
        float r, bb;
        ln_coef(s0[0], diag(q0), r, bb);
        if (G == (t >> 2)) *(LAS f32x2*)(C_R + (16 * lo + t) * 8) = f32x2{r, bb};
        sq4 = xg_sum(G == (t >> 2) ? diag(q4) : 0.f);
      }
      if constexpr (be == 13) {
        float r2, b2, sm = s4[0], sq = sq4;
        sm += dppf<0x128>(sm); sq += dppf<0x128>(sq); sm += dppf<0x124>(sm); sq += dppf<0x124>(sq);
        ln_coef(sm, sq, r2, b2);
        if (lane < 4) *(LAS f32x2*)(C_R + (64 + t4) * 8) = f32x2{r2, b2};
      }
    };
    UNROLL_SEQ(step, 16);
    vmwait<6>();
    __builtin_amdgcn_s_setprio(1);
    BARRIER();
    STAMP(2);
#pragma unroll
    for (int ks = 2; ks < 6; ++ks) w1load(ks, (ks + 4) % 6);
    woread();
    VMWAIT0();
    lflag_set(FL_W1L + lo, 1);
    lwait4(FL_W1L, 1);
#pragma unroll
    for (int r = 0; r < 6; ++r) w1read(r, (r + 4) % 6);
    lflag_set(FL_W1C + lo, 1);
    wcell_dma_half(w2s, wev, L_W2 + lo * 4096, 0); wcell_dma_half(w2s, wev, L_W2 + lo * 4096, 1);
    lwait4(FL_W1C, 1);
    STAMP(23);
#pragma unroll
    for (int ks = 6; ks < 12; ++ks) w1load(ks, ks - 6);
    STAMP(3);
    __builtin_amdgcn_s_setprio(0);
  }
  if (P1ONLY) { VMWAIT0(); STAMP(8); STAMP_OUT(); return; }
  BARRIER();
  v4u w2[11][2];
  const u32 r2a = lo * 4096 + (ro ^ (hi * 64)), r2b = r2a ^ 32;
  auto w2read = [&](int s, u32 row) { w2[s][0] = trfrag(r2a + row, 512); w2[s][1] = trfrag(r2b + row, 512); };
  auto w2half = [&](int s, u32 row, int h) { wcell_dma_half(w2s + ((s == 10 && lo == 3) ? 9 : s) * 32768, wev, row + lo * 4096, h); };
  v4u hres;
  {
    poll8(gw, G_FLA + lo * 8 * FSP, tag, lane);
    STAMP(4);
    const u32 c0 = hi * 32768 + lo * 4096;
#pragma unroll
    for (int i = 0; i < 2; ++i) dma4_sc1(gw + G_O + c0 + i * 16384, lane * 16, c0 + i * 16384);
    BARRIER();
    if (hi) { w1read(6, 0); w1read(7, 1); } else { w1read(0, 2); w1read(1, 3); w1read(2, 4); w1read(3, 5); }
    w2read(0, L_W2);
    v4u bf[2][4];
    f32x4 acc[2][4];
    u32 oa = c0 + lane * 8, ob = oa + 512;
    asm volatile("" : "+v"(oa), "+v"(ob));
    vmwait<4>();
    BARRIER();
    STAMP(9);
#pragma unroll
    for (int s = 0; s < 4; ++s) bf[0][s] = act_frag(oa, ob, s * 1024);
    SB0();
#pragma unroll
    for (int s = 0; s < 2; ++s)
#pragma unroll
      for (int c = 0; c < 4; ++c) acc[0][c] = mfma32(wo[s].f[c], bf[0][s], s == 0 ? zero4 : acc[0][c]);
    SB0();
    VMWAIT0();
#pragma unroll
    for (int s = 0; s < 4; ++s) bf[1][s] = act_frag(oa, ob, 16384 + s * 1024);
    SB0();
#pragma unroll
    for (int s = 2; s < 4; ++s)
#pragma unroll
      for (int c = 0; c < 4; ++c) acc[0][c] = mfma32(wo[s].f[c], bf[0][s], acc[0][c]);
    SB0();
    w1load(12 + 2 * hi, 2 * hi);
    SB0();
#pragma unroll
    for (int s = 0; s < 2; ++s)
#pragma unroll
      for (int c = 0; c < 4; ++c) acc[1][c] = mfma32(wo[s].f[c], bf[1][s], s == 0 ? zero4 : acc[1][c]);
#pragma unroll
    for (int c = 0; c < 4; ++c) *(LAS f32x4*)(c0 + c * 1024 + lane * 16) = acc[0][c];
    SB0();
    w1load(13 + 2 * hi, 2 * hi + 1);
    SB0();
#pragma unroll
    for (int s = 2; s < 4; ++s)
#pragma unroll
      for (int c = 0; c < 4; ++c) acc[1][c] = mfma32(wo[s].f[c], bf[1][s], acc[1][c]);
#pragma unroll
    for (int c = 0; c < 4; ++c) *(LAS f32x4*)(c0 + 16384 + c * 1024 + lane * 16) = acc[1][c];
    STAMP(16);
    BARRIER();
    STAMP(17);
    const u32 pb = lo * 16384 + hi * 2048 + lane * 16;
    f32x4 hv[2];
#pragma unroll
    for (int c = 0; c < 2; ++c)
      hv[c] = ((ldsf4(pb + c * 1024) + ldsf4(pb + 4096 + c * 1024)) + (ldsf4(pb + 8192 + c * 1024) + ldsf4(pb + 12288 + c * 1024))) + unpk4(xres[c].x, xres[c].y);
    const v2u h0 = pk4(hv[0]), h1 = pk4(hv[1]);
    hres = v4u{h0.x, h0.y, h1.x, h1.y};
    __builtin_amdgcn_raw_buffer_store_b128(hres, rws, lane * 16, G_H + (lo * 16 + 2 * j + hi) * 1024, 16);
    SB0();
    const f32x4 s4 = hv[0] + hv[1], q4 = hv[0] * hv[0] + hv[1] * hv[1];
    const float sm = xg_sum((s4[0] + s4[1]) + (s4[2] + s4[3])), sq = xg_sum((q4[0] + q4[1]) + (q4[2] + q4[3]));
    if (lane < 16) __builtin_amdgcn_raw_buffer_store_b64(v2u{asu(sm), asu(sq)}, rws, lane * 8, G_ST + (j >> 1) * 2048 + lo * 512 + ((j & 1) * 2 + hi) * 128, 16);
    SB0();
  }
  STAMP(20);
  w2half(1, L_W2, hi); w2half(2, L_RING + 4 * SLOT, hi);
  f32x4 cs[3] = {zero4, zero4, zero4};
  auto w1sum = [&](int r) {
#pragma unroll
    for (int u = 0; u < 3; ++u) cs[u] = mfma32(w1[r][u], ones, cs[u]);
  };
  w1sum(0); w1sum(1); w1sum(2); w1sum(3);
  vmwait<4>();
  if (lane < FLW) __hip_atomic_store((GAS u64*)(gw + G_FLB + ((lo & 1) * 32 + j * 4 + (lo >> 1) * 2 + hi) * FSP + lane * 8), tag, __ATOMIC_RELAXED, __HIP_MEMORY_SCOPE_AGENT);
  MEMFENCE();
  STAMP(5);
  vmwait<1>();
  BARRIER();
  if (hi == 0) { w1read(4, 0); w1read(5, 1); w1read(6, 2); w1read(7, 3); }
  w2read(1, L_W2); w2read(2, L_RING + 4 * SLOT);
  BARRIER();
#pragma unroll
  for (int s = 0; s < 5; ++s) w2half(3 + s, L_W2 + s * 16384, hi);
  w1sum(4); w1sum(5); w1sum(6); w1sum(7);
  poll8(gw, G_FLB + (hi * 32 + lo * 8) * FSP, tag, lane);
  STAMP(6);
  dma4_sc1(gw + G_H + (hi * 16 + 4 * lo) * 1024, lane * 16, hi * 16384 + lo * 4096);
  dma4_sc1(gw + G_H + ((hi + 2) * 16 + 4 * lo) * 1024, lane * 16, (hi + 2) * 16384 + lo * 4096);
  BARRIER();
  dma1_sc1(gw + G_ST + w * 1024, lane * 16, C_S2 + w * 1024);
#pragma unroll
  for (int s = 0; s < 5; ++s) w2read(3 + s, L_W2 + s * 16384);
  STAMP(21);
  if (t == 0) {
#pragma unroll
    for (int u = 0; u < 3; ++u) *(LAS f32x4*)(C_CS1 + (w * 3 + u) * 64 + G * 16) = cs[u];
  }
  vmwait<5>();
  BARRIER();
  STAMP(7); CSTAMP(29);

  const u32 fco = G_FLC + (j * 4 + lo) * FSP;
  auto fpub = [&](int i) { if (lane < FLW) __hip_atomic_store((GAS u64*)(gw + fco + i * FBL + lane * 8), tag, __ATOMIC_RELAXED, __HIP_MEMORY_SCOPE_AGENT); MEMFENCE(); };
  auto fhalf = [&](int i, u32 slot, int h) {
    const char* src = gw + G_F + i * F_TAU + lo * FPART;
    const u32 dst = slot + lo * FPART;
    if (h == 0) { dma5_sc1(src + 2048, lane * 16, dst + 2048); dma_morem2048_sc1(src + 2048, lane * 16); }
    else if (lo < 3) dma5_sc1(src + 7168, lane * 16, dst + 7168);
    else dma4_sc1(src + 6144, lane * 16, dst + 6144);
  };
  const u32 fl01 = G_FLC + lo * 8 * FSP + (lane & 7) * FSP + ((lane >> 3) & 1) * FBL;
  u64 pq;
  bool got01;
  {
    constexpr int PD = 3;
    v4u hf[32 + PD];
    f32x4 acc[4][3], pv[3], c4[3];
    bool pub01 = false;
    f32x2 sa[2][4];
    float rr[2], bb[2];
    u32 hA = (1 - hi) * (8192 + 16384) + lane * 16, hB = (1 - hi) * 8192 + hi * 16384 + lane * 16;
    u32 pxw = w * 3072 + lane * 16, pxr = (w ^ 4) * 3072 + lane * 16;
    u32 csa = C_CS1 + lo * 192 + G * 16, sta = C_S2 + G * 2048 + hi * 512 + t * 8;
    asm volatile("" : "+v"(hA), "+v"(hB), "+v"(csa), "+v"(sta));
    auto hrd = [&](int n) { return lds128((((n >> 3) & 1) ? hB : hA) + (n >> 4) * 32768 + (n & 7) * 1024); };
    auto ep_load = [&](int sbk) {
      const u32 pxa = pxr + (sbk ? 0 : L_P3);
      if (sbk == 0) {
#pragma unroll
        for (int k = 0; k < 4; ++k) sa[0][k] = ldsf2(sta + k * 128);
      }
#pragma unroll
      for (int u = 0; u < 3; ++u) pv[u] = ldsf4(pxa + u * 1024);
    };
    auto ep_coef = [&](int k) { const f32x2 a_ = (sa[k][0] + sa[k][1]) + (sa[k][2] + sa[k][3]); ln_coef(xg_sum(a_[0]), xg_sum(a_[1]), rr[k], bb[k]); };
    auto ep_out = [&](int sbk, int u) {
      f32x4 v = (acc[2 * sbk + 1][u] + pv[u]) * rr[sbk] + c4[u] * bb[sbk];
      const v2u fo = {pk2(fmaxf(v[0], 0.f), fmaxf(v[1], 0.f)), pk2(fmaxf(v[2], 0.f), fmaxf(v[3], 0.f))};
      if (u < 2 || nv == 3) __builtin_amdgcn_raw_buffer_store_b64(fo, rws, lane * 8, G_F + (2 * sbk + hi) * F_TAU + (Tj + lt + u) * 512, 16);
    };
#pragma unroll
    for (int k = 0; k < PD; ++k) hf[k] = hrd(k);
    auto p3step = [&](auto I) {
      constexpr int n = decltype(I)::value, i = n >> 3, r = n & 7;
      acc[i][0] = mfma32(w1[r][0], hf[n], r == 0 ? zero4 : acc[i][0]);
      SB0();
      if constexpr (n + PD < 32 && !(n < 16 && n + PD >= 16)) hf[n + PD] = hrd(n + PD);
      if constexpr (n == 0 || n == 12) {
        if (hi == (n ? 1 : 0)) { w2half(8, L_W2, hi); w2half(9, L_W2 + 16384, hi); w2half(10, L_W2 + 32768, hi); }
      }
      if constexpr (n == 12) {
#pragma unroll
        for (int u = 0; u < 3; ++u) c4[u] = ldsf4(csa + u * 64) + ldsf4(csa + 768 + u * 64);
      }
      if constexpr (n == 10 || n == 26) {
#pragma unroll
        for (int u = 0; u < 3; ++u) *(LAS f32x4*)(pxw + (n == 10 ? L_P3 : 0) + u * 1024) = acc[i - 1][u];
      }
      if constexpr (n == 16) ep_coef(0);
      if constexpr (n >= 17 && n <= 19) ep_out(0, n - 17);
      if constexpr (n == 21) {
#pragma unroll
        for (int k = 0; k < 4; ++k) sa[1][k] = ldsf2(sta + 1024 + k * 128);
      }
      if constexpr (n == 23) ep_coef(1);
      if constexpr (n == 27 || n == 28 || n == 30) w2read(8 + (n == 27 ? 0 : n == 28 ? 1 : 2), L_W2 + (n == 27 ? 0 : n == 28 ? 1 : 2) * 16384);
      if constexpr (n == 23 || n == 25) { if (!pub01 && vm_idle()) { VMWAIT0(); fpub(hi); pub01 = true; } }
      if constexpr (n == 27) { if (!pub01) { VMWAIT0(); fpub(hi); } }
      SB0();
      acc[i][1] = mfma32(w1[r][1], hf[n], r == 0 ? zero4 : acc[i][1]);
      acc[i][2] = mfma32(w1[r][2], hf[n], r == 0 ? zero4 : acc[i][2]);
      SB0();
      if constexpr (n == 21) { if (nv == 3) vmwait<3>(); else vmwait<2>(); BARRIER(); }
      if constexpr (n == 15) {
        vmwait<6>();
        BARRIER();
        STAMP(26); CSTAMP(30);
#pragma unroll
        for (int k = 0; k < PD; ++k) hf[16 + k] = hrd(16 + k);
        ep_load(0);
        SB0();
      }
    };
    UNROLL_SEQ(p3step, 32);
    BARRIER();
    STAMP(22);
    poll_issue(pq, gw, fl01);
    ep_load(1);
#pragma unroll
    for (int u = 0; u < 3; ++u) ep_out(1, u);
    SB0();
    if (nv == 3) poll_wait<3>(pq); else poll_wait<2>(pq);
    got01 = (__builtin_amdgcn_ballot_w64(pq == tag) & 0xffff) == 0xffff;
    MEMFENCE();
  }
  STAMP(18);
  {
    auto poll16 = [&](u32 off) { const u64 v = __hip_atomic_load((GAS u64*)(gw + off), __ATOMIC_RELAXED, __HIP_MEMORY_SCOPE_AGENT); const bool r = (__builtin_amdgcn_ballot_w64(v == tag) & 0xffff) == 0xffff; MEMFENCE(); return r; };
    if (got01) {
      fhalf(0, L_F1, hi); fhalf(1, L_F2, hi);
      P4S(1);
      vmwait<8>(); fpub(2 + hi);
      STAMP(19);
      vmwait<5>();
    } else {
      bool g1 = poll16(fl01);
      VMWAIT0(); fpub(2 + hi);
      STAMP(19);
#pragma unroll 1
      for (u32 s = 0; s < SPIN_G && !g1; ++s) g1 = poll16(fl01);
      fhalf(0, L_F1, hi); fhalf(1, L_F2, hi);
      vmwait<4>();
    }
    STAMP(14);
    f32x4 acc[4][2];
    auto tile = [&](int i, u32 slot) {
      u32 sa = slot + lo * FPART + lane * 8, sb = sa + 512;
      asm volatile("" : "+v"(sa), "+v"(sb));
      constexpr int PD = 4;
      v4u bf[11];
      SB0();
#pragma unroll
      for (int k = 0; k < PD; ++k) bf[k] = act_frag(sa, sb, k * 1024);
      const u32 mk = (i == lo) ? 0xffffffffu : 0u;
      acc[i][0] = unpk4(hres.x & mk, hres.y & mk); acc[i][1] = unpk4(hres.z & mk, hres.w & mk);
      SB0();
#pragma unroll
      for (int k = 0; k < 10; ++k) {
        acc[i][0] = mfma32(w2[k][0], bf[k], acc[i][0]);
        SB0();
        if (k + PD < 11) bf[k + PD] = act_frag(sa, sb, (k + PD) * 1024);
        SB0();
        acc[i][1] = mfma32(w2[k][1], bf[k], acc[i][1]);
        SB0();
      }
      if (lo < 3) { acc[i][0] = mfma32(w2[10][0], bf[10], acc[i][0]); acc[i][1] = mfma32(w2[10][1], bf[10], acc[i][1]); }
      SB0();
    };
    const u32 pw = lo * PQ + t * 272 + hi * 128 + G * 16;
    auto dump = [&](int i, u32 buf) { *(LAS f32x4*)(buf + pw) = acc[i][0]; *(LAS f32x4*)(buf + pw + 64) = acc[i][1]; };
    const u32 pr = (2 * w + (lane >> 5)) * 272 + (lane & 31) * 8;
    char* const op = p.out + (size_t)(T0 + 2 * w) * 1024 + j * 128;
    const u32 ov = (lane >> 5) * 1024 + (lane & 31) * 4;
    auto fin = [&](int i, u32 buf) {
      const f32x2 sv = (ldsf2(buf + pr) + ldsf2(buf + pr + PQ)) + (ldsf2(buf + pr + 2 * PQ) + ldsf2(buf + pr + 3 * PQ));
      *(u32*)(op + i * 16384 + ov) = pk2(sv[0], sv[1]);
    };
    BARRIER();
    STAMP(12);
    pq = __hip_atomic_load((GAS u64*)(gw + fl01 + 2 * FBL), __ATOMIC_RELAXED, __HIP_MEMORY_SCOPE_AGENT);
    tile(0, L_F1);
    P4S(2);
    dump(0, L_P4);
    const bool got = (__builtin_amdgcn_ballot_w64(pq == tag) & 0xffff) == 0xffff;
    VMWAIT0();
    P4S(3);
    if (got) { fhalf(2, 0, hi); P4S(4); }
    BARRIER();
    P4S(5);
    if (got) { if (hi == 0) fhalf(3, L_F1, 0); }
    else pq = __hip_atomic_load((GAS u64*)(gw + fl01 + 2 * FBL), __ATOMIC_RELAXED, __HIP_MEMORY_SCOPE_AGENT);
    fin(0, L_P4);
    P4S(6);
    tile(1, L_F2);
    P4S(7);
    if (got) { if (hi == 1) fhalf(3, L_F1, 1); }
    else {
      bool g2 = (__builtin_amdgcn_ballot_w64(pq == tag) & 0xffff) == 0xffff;
      MEMFENCE();
#pragma unroll 1
      for (u32 s = 0; s < SPIN_G && !g2; ++s) g2 = poll16(fl01 + 2 * FBL);
      fhalf(2, 0, hi); fhalf(3, L_F1, hi);
    }
    STAMP(15);
    vmwait<4>();
    BARRIER();
    STAMP(13);
    dump(1, L_P4);
    tile(2, 0);
    P4S(9);
    dump(2, L_F2);
    VMWAIT0();
    BARRIER();
    P4S(16);
    fin(1, L_P4); fin(2, L_F2);
    P4S(17);
    tile(3, L_F1);
    P4S(20);
    dump(3, L_F2 + 20480);
    BARRIER();
    fin(3, L_F2 + 20480);
  }
  if (w == 0 && j == 0 && lane == 0) __hip_atomic_store((GAS u64*)(gw + G_EPOCH), tag, __ATOMIC_RELAXED, __HIP_MEMORY_SCOPE_AGENT);
  STAMP(8); CSTAMP(11);
  STAMP_OUT();
}
}

extern "C" void kernel_launch(void* const* d_in, const int* in_sizes, int n_in, void* d_out, int out_size, void* d_ws, size_t ws_size,
                              hipStream_t stream) {
  fb::Params p;
  p.x = (const char*)d_in[0]; p.wqkv = (const char*)d_in[1]; p.wo = (const char*)d_in[2];
  p.w1 = (const char*)d_in[4]; p.w2 = (const char*)d_in[5];
  p.out = (char*)d_out; p.ws = (char*)d_ws;
  (void)hipFuncSetAttribute((const void*)fb::fused_block, hipFuncAttributeMaxDynamicSharedMemorySize, fb::L_TOTAL);
  fb::fused_block<<<256, 512, fb::L_TOTAL, stream>>>(p);
}
```

```cpp
#include <hip/hip_runtime.h>
#include <hip/hip_bf16.h>
#include <utility>
#pragma clang diagnostic ignored "-Wint-to-pointer-cast"
#pragma clang diagnostic ignored "-Wc++20-extensions"
#define MAPC 1
#define TOUCH 19
#define TW 2
#define TSTEP 65536u
#define TO 5
#define FLW 1
#define APRIO0 0
#define OUTSC 0
#define PSL 0
#define ONEPOLL 1
#define CODE_KB 30
#define AF1 0
#define VIDLE 3
#define SCALEP 0
#define P1ONLY 0
#define P4S(i)
namespace fb {
typedef unsigned u32; typedef unsigned long long u64; typedef unsigned short u16;
typedef u32 v4u __attribute__((ext_vector_type(4)));
typedef u32 v2u __attribute__((ext_vector_type(2)));
typedef float f32x4 __attribute__((ext_vector_type(4)));
typedef float f32x2 __attribute__((ext_vector_type(2)));
typedef __bf16 bf8 __attribute__((ext_vector_type(8)));
typedef __bf16 bf2 __attribute__((ext_vector_type(2)));
typedef short s4 __attribute__((ext_vector_type(4)));
#define LAS __attribute__((address_space(3)))
#define GAS __attribute__((address_space(1)))
#define DEV __device__ __forceinline__
#define IC(x) std::integral_constant<int, (x)>{}
#define UNROLL_SEQ(fn, N) [&]<int... Is_>(std::integer_sequence<int, Is_...>) { (fn(std::integral_constant<int, Is_>{}), ...); }(std::make_integer_sequence<int, N>{})

constexpr u32 G_EPOCH = 0;
constexpr u32 G_O = 8192;
constexpr u32 G_H = G_O + 65536;
constexpr u32 G_F = G_H + 65536;
constexpr u32 F_TAU = 86 * 512;
constexpr u32 G_ST = 315392 + 1024;
constexpr u32 FSP = 256, FBL = 32 * FSP;
constexpr u32 G_FLA = 327680, G_FLB = G_FLA + 2 * FBL, G_FLC = G_FLB + 2 * FBL, G_BYTES = G_FLC + 4 * FBL;
static_assert(4 * G_BYTES <= (2u << 20) && G_ST + 8192 <= G_FLA, "workspace map");
__host__ __device__ constexpr size_t group_base(int g) { return (size_t)(g >> 2) * (2u << 20) + (size_t)(g & 3) * G_BYTES; }
constexpr u32 WS_DBG = 16u << 20;
constexpr u32 L_X = 0, L_RING = 81920, SLOT = 12288, L_CTRL = 155648, L_TOTAL = 163840;
constexpr u32 L_W2 = 65536, L_P3 = 114688, C_S2 = 147456;
constexpr u32 FPART = 11264, L_F1 = 65536, L_F2 = 110592, L_P4 = 45056, PQ = 4352;
constexpr u32 C_R = L_CTRL;
constexpr u32 C_CS1 = L_CTRL + 1024;
constexpr u32 C_FLAGS = L_CTRL + 768, FL_W1L = 0, FL_W1C = 4, FL_COEF = 8, FL_W2D = 12, FL_BDONE = 16, FL_FRDY = 20, FL_A0 = 24, C_TAG = L_CTRL + 640, NSLOT = 6;
constexpr u32 C_TB = L_CTRL + 2048, C_HR = L_CTRL + 4096, AWP = 22528, AWW = 20480;
constexpr u32 SPIN_G = 1u << 16, SPIN_L = 1u << 20;

struct Params { const char* x; const char* wqkv; const char* wo; const char* w1; const char* w2; char* out; char* ws; };

DEV f32x4 mfma32(v4u a, v4u b, f32x4 c) { return __builtin_amdgcn_mfma_f32_16x16x32_bf16(__builtin_bit_cast(bf8, a), __builtin_bit_cast(bf8, b), c, 0, 0, 0); }
DEV f32x4 mfma16(v2u a, v2u b, f32x4 c) { return __builtin_amdgcn_mfma_f32_16x16x16bf16_1k(__builtin_bit_cast(s4, a), __builtin_bit_cast(s4, b), c, 0, 0, 0); }
DEV void mfma32t(f32x4& c, v4u a, v4u b) { asm volatile("v_mfma_f32_16x16x32_bf16 %0, %1, %2, %0" : "+v"(c) : "v"(a), "v"(b)); }
DEV void mfma32z(f32x4& c, v4u a, v4u b) { asm volatile("v_mfma_f32_16x16x32_bf16 %0, %1, %2, 0" : "=&v"(c) : "v"(a), "v"(b)); }
DEV u32 pk2(float a, float b) { bf2 p = {(__bf16)a, (__bf16)b}; return __builtin_bit_cast(u32, p); }
DEV v2u pk4(f32x4 v) { return v2u{pk2(v[0], v[1]), pk2(v[2], v[3])}; }
DEV float asf(u32 u) { return __builtin_bit_cast(float, u); }
DEV u32 asu(float f) { return __builtin_bit_cast(u32, f); }
DEV f32x4 unpk4(u32 lo, u32 hi) { return f32x4{asf(lo << 16), asf(lo & 0xffff0000u), asf(hi << 16), asf(hi & 0xffff0000u)}; }
DEV u32 uni(u32 v) { return __builtin_amdgcn_readfirstlane(v); }
DEV v4u lds128(u32 a) { return *(const LAS v4u*)a; }
DEV v2u lds64(u32 a) { return *(const LAS v2u*)a; }
DEV f32x4 ldsf4(u32 a) { return *(const LAS f32x4*)a; }
DEV f32x2 ldsf2(u32 a) { return *(const LAS f32x2*)a; }
DEV float ldsf(u32 a) { return *(const LAS float*)a; }
DEV v2u ldstr(u32 a) { return __builtin_bit_cast(v2u, __builtin_amdgcn_ds_read_tr16_b64_v4i16((LAS s4*)a)); }
DEV float vmax(float a, float b) { float r; asm("v_max_f32 %0, %1, %2" : "=v"(r) : "v"(a), "v"(b)); return r; }
#define MEMFENCE() asm volatile("" ::: "memory")
#define SB0() __builtin_amdgcn_sched_barrier(0)
#define BARRIER() do { SB0(); asm volatile("s_waitcnt lgkmcnt(0)\n\ts_barrier" ::: "memory"); SB0(); } while (0)
#define SGB(m, n) __builtin_amdgcn_sched_group_barrier(m, n, 0)
#define VMWAIT0() asm volatile("s_waitcnt vmcnt(0)" ::: "memory")
#define LGWAIT0() asm volatile("s_waitcnt lgkmcnt(0)" ::: "memory")
template <int N> DEV void vmwait() { asm volatile("s_waitcnt vmcnt(%0)" ::"i"(N) : "memory"); }
#define GLD "global_load_lds_dwordx4 "
DEV void dma3(const char* sb, u32 v, u32 dst) {
  asm volatile("s_mov_b32 m0, %2\n\ts_nop 0\n\t" GLD "%0, %1\n\t" GLD "%0, %1 offset:1024\n\t" GLD "%0, %1 offset:2048" ::"v"(v), "s"(sb), "s"(dst) : "memory");
}
DEV void dma2v(const char* sb, u32 v0, u32 v1, u32 dst) {
  asm volatile("s_mov_b32 m0, %3\n\ts_nop 0\n\t" GLD "%0, %2\n\t" GLD "%1, %2 offset:1024" ::"v"(v0), "v"(v1), "s"(sb), "s"(dst) : "memory");
}
DEV void dma3v(const char* sb, u32 v0, u32 v1, u32 v2, u32 dst) {
  asm volatile("s_mov_b32 m0, %4\n\ts_nop 0\n\t" GLD "%0, %3\n\t" GLD "%1, %3 offset:1024\n\t" GLD "%2, %3 offset:2048"
               ::"v"(v0), "v"(v1), "v"(v2), "s"(sb), "s"(dst) : "memory");
}
DEV void dma4v(const char* sb, u32 v0, u32 v1, u32 v2, u32 v3, u32 dst) {
  asm volatile("s_mov_b32 m0, %5\n\ts_nop 0\n\t" GLD "%0, %4\n\t" GLD "%1, %4 offset:1024\n\t" GLD "%2, %4 offset:2048\n\t" GLD "%3, %4 offset:3072"
               ::"v"(v0), "v"(v1), "v"(v2), "v"(v3), "s"(sb), "s"(dst) : "memory");
}
DEV void dma4w(const char* sb, u32 v0, u32 v1, u32 v2, u32 v3, u32 dst) {
  asm volatile("s_mov_b32 m0, %5\n\ts_nop 0\n\t" GLD "%0, %4\n\t" GLD "%1, %4 offset:1152\n\t" GLD "%2, %4 offset:2048\n\t" GLD "%3, %4 offset:3200"
               ::"v"(v0), "v"(v1), "v"(v2), "v"(v3), "s"(sb), "s"(dst) : "memory");
}
DEV void dma_more2048(const char* sb, u32 v) { asm volatile(GLD "%0, %1 offset:2048" ::"v"(v), "s"(sb) : "memory"); }
DEV void touch(const char* sb, u32 v) {
  asm volatile("s_mov_b32 m0, %2\n\ts_nop 0\n\tglobal_load_lds_dword %0, %1" ::"v"(v), "s"(sb), "s"(L_CTRL + 3072) : "memory");
}
DEV void dma1_sc1(const char* sb, u32 v, u32 dst) {
  asm volatile("s_mov_b32 m0, %2\n\ts_nop 0\n\t" GLD "%0, %1 sc1" ::"v"(v), "s"(sb), "s"(dst) : "memory");
}
DEV void dma4_sc1(const char* sb, u32 v, u32 dst) {
  asm volatile("s_mov_b32 m0, %2\n\ts_nop 0\n\t" GLD "%0, %1 sc1\n\t" GLD "%0, %1 offset:1024 sc1\n\t" GLD "%0, %1 offset:2048 sc1\n\t"
               GLD "%0, %1 offset:3072 sc1" ::"v"(v), "s"(sb), "s"(dst) : "memory");
}
DEV void dma8_sc1(const char* sb, u32 v, u32 dst) {
  asm volatile("s_mov_b32 m0, %2\n\ts_nop 0\n\t" GLD "%0, %1 offset:-4096 sc1\n\t" GLD "%0, %1 offset:-3072 sc1\n\t" GLD "%0, %1 offset:-2048 sc1\n\t"
               GLD "%0, %1 offset:-1024 sc1\n\t" GLD "%0, %1 sc1\n\t" GLD "%0, %1 offset:1024 sc1\n\t" GLD "%0, %1 offset:2048 sc1\n\t"
               GLD "%0, %1 offset:3072 sc1" ::"v"(v), "s"(sb), "s"(dst) : "memory");
}
DEV void dma2_sc1(const char* sb, u32 v, u32 dst) {
  asm volatile("s_mov_b32 m0, %2\n\ts_nop 0\n\t" GLD "%0, %1 sc1\n\t" GLD "%0, %1 offset:1024 sc1" ::"v"(v), "s"(sb), "s"(dst) : "memory");
}
DEV void dma_more2048_sc1(const char* sb, u32 v) { asm volatile(GLD "%0, %1 offset:2048 sc1" ::"v"(v), "s"(sb) : "memory"); }
DEV void lflag_set(u32 word, u32 v) {
  MEMFENCE();
  __hip_atomic_store((LAS u32*)(C_FLAGS + word * 4), v, __ATOMIC_RELAXED, __HIP_MEMORY_SCOPE_WORKGROUP);
  MEMFENCE();
}
DEV u32 lflag_min4(u32 word) {
  v4u v;
  asm volatile("ds_read_b128 %0, %1\n\ts_waitcnt lgkmcnt(0)" : "=v"(v) : "v"(C_FLAGS + word * 4) : "memory");
  u32 m = min(min(v.x, v.y), min(v.z, v.w));
  return uni(m);
}
DEV u32 lflag_get(u32 word) {
  u32 v;
  asm volatile("ds_read_b32 %0, %1\n\ts_waitcnt lgkmcnt(0)" : "=v"(v) : "v"(C_FLAGS + word * 4) : "memory");
  return uni(v);
}
DEV void lwait4(u32 word, u32 want) {
#pragma unroll 1
  for (u32 s = 0; s < SPIN_L && lflag_min4(word) < want; ++s) {} }
DEV void lwait1(u32 word, u32 want) {
#pragma unroll 1
  for (u32 s = 0; s < SPIN_L && lflag_get(word) < want; ++s) {} }
DEV void gflag_pub(char* gw, u32 off, u64 tag, int lane) {
  VMWAIT0();
  if (lane < FLW) __hip_atomic_store((GAS u64*)(gw + off + lane * 8), tag, __ATOMIC_RELAXED, __HIP_MEMORY_SCOPE_AGENT);
  MEMFENCE();
}
DEV u64 gflag_poll(char* gw, u32 off, u64 tag, int lane, int n) {
  u64 v = __hip_atomic_load((GAS u64*)(gw + off + (lane & 7) * FSP + ((lane & (n - 1)) >> 3) * (32 * FSP)), __ATOMIC_RELAXED, __HIP_MEMORY_SCOPE_AGENT);
  u64 m = __builtin_amdgcn_ballot_w64(v == tag);
  MEMFENCE();
  return m;
}
DEV bool vm_idle() { return (__builtin_amdgcn_s_getreg((31 << 11) | 7) & 0xC0000Fu) == 0; }
DEV u64 mix64(u64 z) {
  z += 0x9E3779B97F4A7C15ull; z = (z ^ (z >> 30)) * 0xBF58476D1CE4E5B9ull; z = (z ^ (z >> 27)) * 0x94D049BB133111EBull; return z ^ (z >> 31);
}
DEV float xg_sum(float v) {
  auto a = __builtin_amdgcn_permlane16_swap(asu(v), asu(v), false, false);
  v = asf(a[0]) + asf(a[1]);
  auto b = __builtin_amdgcn_permlane32_swap(asu(v), asu(v), false, false);
  return asf(b[0]) + asf(b[1]);
}
template <int CTRL> DEV float dppf(float v) { return asf(__builtin_amdgcn_update_dpp(0u, asu(v), CTRL, 0xf, 0xf, false)); }
DEV void ln_coef(float s, float q, float& r, float& b) {
  float m = s * (1.0f / 512.0f);
  float var = fmaxf(q * (1.0f / 512.0f) - m * m, 0.0f);
  r = __builtin_amdgcn_rsqf(var + 1e-5f);
  b = -m * r;
}
#define STAMP(i)
#define CSTAMP(i)
#define STAMP_DECL
#define STAMP_OUT()

struct Frag4 { v4u f[4]; };
DEV void wcell_dma_half(const char* src, u32 ve, u32 cell, int h) { dma2v(src + h * 8192, ve, (ve ^ 64) + 3072, cell + h * 2048); }
DEV u32 wcell_voff(int lane, int j) {
  const int r = lane >> 3;
  return (16 * (r >> 2) + (r & 3)) * 1024 + j * 128 + ((((lane & 7) >> 1) ^ ((r >> 1) & 1)) * 32) + (lane & 1) * 16;
}
DEV u32 wcell_roff(int lane) {
  const int G = lane >> 4, q = (lane & 15) >> 2;
  return G * 1024 + q * 128 + ((((q >> 1) & 1) | ((G & 1) << 1)) * 32) + (lane & 3) * 8;
}
DEV v4u trfrag(u32 ad, u32 d2) { v2u lo = ldstr(ad), hi = ldstr(ad + d2); return v4u{lo.x, lo.y, hi.x, hi.y}; }
DEV v4u act_frag(u32 a, u32 a2, u32 off) { v2u lo = lds64(a + off), hi = lds64(a2 + off); return v4u{lo.x, lo.y, hi.x, hi.y}; }
DEV void dma1(const char* sb, u32 v, u32 dst) { asm volatile("s_mov_b32 m0, %2\n\ts_nop 0\n\t" GLD "%0, %1" ::"v"(v), "s"(sb), "s"(dst) : "memory"); }
DEV void dma5_sc1(const char* sb, u32 v, u32 dst) {
  asm volatile("s_mov_b32 m0, %2\n\ts_nop 0\n\t" GLD "%0, %1 offset:-1024 sc1\n\t" GLD "%0, %1 sc1\n\t" GLD "%0, %1 offset:1024 sc1\n\t"
               GLD "%0, %1 offset:2048 sc1\n\t" GLD "%0, %1 offset:3072 sc1" ::"v"(v), "s"(sb), "s"(dst) : "memory");
}
DEV void dma_morem2048_sc1(const char* sb, u32 v) { asm volatile(GLD "%0, %1 offset:-2048 sc1" ::"v"(v), "s"(sb) : "memory"); }
DEV void touch64(const char* ad) { asm volatile("s_mov_b32 m0, %1\n\ts_nop 0\n\tglobal_load_lds_dword %0, off sc1" ::"v"(ad), "s"(L_CTRL + 3072) : "memory"); }
DEV void poll_issue(u64& v, const char* sb, u32 off) { asm volatile("global_load_dwordx2 %0, %1, %2 sc1" : "=v"(v) : "v"(off), "s"(sb) : "memory"); }
template <int N> DEV void poll_wait(u64& v) { asm volatile("s_waitcnt vmcnt(%1)" : "+v"(v) : "i"(N) : "memory"); }
DEV void poll8(char* gw, u32 off, u64 tag, int lane) {
#pragma unroll 1
  for (u32 s = 0; s < SPIN_G && (gflag_poll(gw, off, tag, lane, 8) & 0xff) != 0xff; ++s) { if (PSL) __builtin_amdgcn_s_sleep(PSL); }
}

constexpr int SCH[17][4] = {{1, 2, 3, 4},     {5, 18, -1, -1},  {6, 19, -1, -1},  {7, 20, -1, -1},  {8, 21, -1, -1},  {9, 22, -1, -1},
                            {10, 23, -1, -1}, {11, -1, -1, -1}, {12, -1, -1, -1}, {13, -1, -1, -1}, {14, -1, -1, -1}, {15, -1, -1, -1},
                            {24, -1, -1, -1}, {25, -1, -1, -1}, {26, -1, -1, -1}, {27, -1, -1, -1}, {30, 31, -1, -1}};
constexpr int item_ops(int it) { return it < 0 ? 0 : it < 16 ? 3 : it < 24 ? ((it & 1) ? 2 : 3) : it < 30 ? 4 : 3; }
constexpr int item_end(int it) { int n = 0; for (int gI = 0; gI < 17; ++gI) for (int k = 0; k < 4; ++k) { n += item_ops(SCH[gI][k]); if (SCH[gI][k] == it) return n; } return 0; }
constexpr int issued_before_barrier(int b) { int n = 0; for (int gI = 0; gI <= b; ++gI) for (int k = 0; k < 4; ++k) n += item_ops(SCH[gI][k]); return n; }
constexpr int xpair_at(int b) { return b < 4 ? 0 : b < 7 ? 1 : b < 9 ? 2 : 3; }
constexpr int need_before_barrier(int b) { return item_end(b) > item_end(17 + 2 * xpair_at(b)) ? item_end(b) : item_end(17 + 2 * xpair_at(b)); }

__global__ void __launch_bounds__(512, 2) fused_block(Params p) {
  extern __shared__ __attribute__((aligned(16))) char smem[];
  const int tid = threadIdx.x, lane = tid & 63;
  const int w = uni(tid >> 6), lo = w & 3, hi = w >> 2;
  const int bid = blockIdx.x, xcd = bid & 7, idx = bid >> 3;
  const int g = xcd * 4 + (idx >> 3), j = idx & 7;
  const int T0 = g * 64;
  const int t = lane & 15, G = lane >> 4, rho = lane >> 3;
  char* const gw = p.ws + group_base(g);
  const __amdgpu_buffer_rsrc_t rws = __builtin_amdgcn_make_buffer_rsrc((void*)gw, 0, G_BYTES, 0x00020000);
  STAMP_DECL;
  STAMP(0); CSTAMP(10);
  const char* pc0;
  asm volatile("s_getpc_b64 %0" : "=s"(pc0));
  asm volatile("" ::"s"(smem));
  const u32 wv = (8 * lo + rho) * 3072 + j * 128 + (((((lane & 7) >> 1) ^ (((rho >> 1) & 1) | ((lo & 1) << 1))) * 32) + (lane & 1) * 16);
  auto wload = [&](int c) { dma3(p.wqkv + (size_t)((c + 2 * xcd) & 15) * (32 * 3072), wv, L_RING + (c % NSLOT) * SLOT + lo * 3072); };
  u32 zz = 0; asm volatile("" : "+v"(zz));
  u64 epv;
  asm volatile("global_load_dwordx2 %0, %1, %2 sc1" : "=v"(epv) : "v"(zz), "s"(gw + G_EPOCH) : "memory");
  if (hi == 0) wload(0); else { wload(1); wload(2); wload(3); wload(4); }
  u32 xv[5];
#pragma unroll
  for (int n = 0; n < 5; ++n) {
    const int i = (5 * lo + n) % 10, row = T0 - 16 + 8 * i + rho;
    xv[n] = max(row, 0) * 1024 + ((((lane & 7) ^ (rho >> 1)) << 4) ^ ((i & 1) * 64)) + 2048 - (n < 3 ? n : n - 3) * 1024;
  }
  auto xload = [&](int P, int part) {
    const char* src = p.x - 2048 + ((2 * P + (lo >> 1) + xcd) & 7) * 128;
    if (part == 0) dma3v(src, xv[0], xv[1], xv[2], L_X + P * 20480 + 5 * lo * 1024);
    else dma2v(src, xv[3], xv[4], L_X + P * 20480 + (5 * lo + 3) * 1024);
  };
  if (hi == 0) { xload(0, 0); xload(0, 1); }
  const v4u ones = {0x3F803F80u, 0x3F803F80u, 0x3F803F80u, 0x3F803F80u};
  const f32x4 zero4 = {0, 0, 0, 0};
  const int Tj = 22 * (j >> 1) + ((j & 1) ? (j < 6 ? 12 : 10) : 0);
  const bool full = !(j & 1) && j < 6;
  const int lt = (full || lo < 3) ? 3 * lo : 8;
  const int nv = (full || lo < 2) ? 3 : 2;
  const u32 wev = wcell_voff(lane, j), ro = wcell_roff(lane);
  u32 w1v[3], ra[3];
  {
    const int r8 = (lane >> 2) & 7, mis = (r8 + (Tj >> 1)) & 1;
#pragma unroll
    for (int n = 0; n < 3; ++n) {
      int q6 = (2 * n + (lane >> 5) + 6 - mis) % 6;
      if (!full && q6 == 5) q6 = mis ? 0 : 4;
      w1v[n] = (16 * (r8 >> 2) + (r8 & 3) + 4 * lo) * 2752 + (Tj + 2 * q6 + (((lane >> 1) ^ lo) & 1)) * 32 + (lane & 1) * 16 + 2048 - n * 1024;
      ra[n] = L_RING + G * 3072 + ((((lt + n) >> 1) + mis) % 6) * 512 + ((lane & 15) >> 2) * 64 + ((((lt + n) ^ G) & 1) * 32) + (lane & 3) * 8;
      asm volatile("" : "+v"(ra[n]));
    }
  }
  auto w1load = [&](int ks, int sl) { dma3v(p.w1 - 2048 + (size_t)(32 * ks) * 2752, w1v[0], w1v[1], w1v[2], L_RING + sl * SLOT + lo * 3072); };
  v4u wr[48];
  auto w1read = [&](int r, int sl) {
#pragma unroll
    for (int u = 0; u < 3; ++u) wr[3 * r + u] = trfrag(ra[u] + sl * SLOT, 256);
  };
  const char* w2s = p.w2 + (size_t)(11 * lo) * 32768;
  auto w2cell = [&](int s, u32 cell) { wcell_dma_half(w2s + s * 32768, wev, cell, 0); wcell_dma_half(w2s + s * 32768, wev, cell, 1); };
  auto w2read = [&](int s, u32 cell) {
#pragma unroll
    for (int k = 0; k < 4; ++k) wr[4 * s + k] = trfrag(cell + (ro ^ (k * 32)), 512);
  };
  Frag4 wo[4];
  auto woread = [&]() {
#pragma unroll
    for (int s = 0; s < 4; ++s)
#pragma unroll
      for (int c = 0; c < 4; ++c) wo[s].f[c] = trfrag(s * 16384 + lo * 4096 + (ro ^ (c * 32)), 512);
  };
  v2u xres[2];
  const u32 xra = L_X + (((j - xcd) & 7) * 5 + lo + 1) * 2048 + (t >> 3) * 1024 + (t & 7) * 128 + (((4 * hi + (G >> 1)) ^ ((t >> 1) & 7)) << 4) + (G & 1) * 8;
  const u32 xl = (t >> 3) * 1024 + (t & 7) * 128 + ((G ^ ((t >> 1) & 7)) << 4);
  u64 tag;
  int hr_ = hi;
  asm volatile("" : "+s"(hr_));

  if (hr_ == 0) {
    __builtin_amdgcn_s_setprio(2);
    if (w == 0 && lane < 32) *(LAS u32*)(C_FLAGS + lane * 4) = 0;
    f32x4 aq[5], ak[5], av[5];
#pragma unroll
    for (int i = 0; i < 5; ++i) { aq[i] = zero4; ak[i] = zero4; av[i] = zero4; asm volatile("" : "+v"(aq[i]), "+v"(ak[i]), "+v"(av[i])); }
    const int q = (lane & 15) >> 2;
    const u32 woff = L_RING + G * 3072 + q * 128 + ((lo ^ (((q >> 1) & 1) | ((G & 1) << 1))) * 32) + (lane & 3) * 8;
    struct Fr { v4u w[3]; v4u x[5]; };
    auto ldfr = [&](Fr& f, u32 wa, u32 xa) {
#pragma unroll
      for (int m = 0; m < 3; ++m) f.w[m] = trfrag(wa + m * 1024, 512);
#pragma unroll
      for (int i = 0; i < 5; ++i) f.x[i] = lds128(xa + i * 2048);
    };
    auto mma = [&](Fr& f) {
      v4u x0 = f.x[0];
      if (t == 0) x0 = ones;
#pragma unroll
      for (int i = 1; i < 5; ++i) { aq[i] = mfma32(f.w[0], f.x[i], aq[i]); ak[i] = mfma32(f.w[1], f.x[i], ak[i]); av[i] = mfma32(f.x[i], f.w[2], av[i]); }
      aq[0] = mfma32(f.w[0], x0, aq[0]); ak[0] = mfma32(f.w[1], x0, ak[0]); av[0] = mfma32(x0, f.w[2], av[0]);
    };
    auto pat = [&]() {
#pragma unroll
      for (int i = 0; i < 6; ++i) { SGB(0x8, 1); SGB(0x100, 2); }
      SGB(0x8, 9);
    };
    Fr f0, f1;
    u32 so = 0, xa = L_X + xl;
    VMWAIT0();
    BARRIER();
    STAMP(1); CSTAMP(24);
    ldfr(f0, woff, xa);
    asm volatile("" : "+v"(epv));
    tag = mix64(((u64)uni((u32)(epv >> 32)) << 32) | uni((u32)epv));
    SB0();
#pragma unroll 1
    for (int it = 0; it < 7; ++it) {
      so = (so == (NSLOT - 1) * SLOT) ? 0 : so + SLOT;
      BARRIER();
      ldfr(f1, woff + so, xa ^ 64);
      mma(f0);
      pat();
      so = (so == (NSLOT - 1) * SLOT) ? 0 : so + SLOT;
      xa += 10240;
      BARRIER();
      ldfr(f0, woff + so, xa);
      mma(f1);
      pat();
      SB0();
      if (it <= 4) { xres[0] = lds64(xra); xres[1] = lds64(xra ^ 32); }
      if (it == TW) {
        const u32 id = bid * 102 + min(lo * 26 + (lane < 26 ? lane : 25), 101);
        touch64(id < 4096 ? p.wo + id * 128 : id < 15104 ? p.w1 + (id - 4096) * 128 : p.w2 + (id - 15104) * 128);
      }
      if (it == 0 && lo == 3) {
        if (lane < 8) touch64(pc0 + (idx * 8 + lane) * (CODE_KB * 4));
      }
      if (it == TO) {
        if (lane < 8) touch64(gw + G_FLA + (lane * 32 + j * 4 + lo) * FSP);
      }
    }
    BARRIER();
    ldfr(f1, woff + 3 * SLOT, xa ^ 64);
    f32x2 rb1[5]; f32x4 rbA[5], rbB[5];
    {
      u32 ca = C_R + t * 8, cb = C_R + G * 32;
      asm volatile("" : "+v"(ca), "+v"(cb));
#pragma unroll
      for (int i = 0; i < 5; ++i) { rb1[i] = ldsf2(ca + i * 128); rbA[i] = ldsf4(cb + i * 128); rbB[i] = ldsf4(cb + i * 128 + 16); }
    }
    mma(f0);
#pragma unroll
    for (int i = 0; i < 13; ++i) { SGB(0x8, 1); SGB(0x100, 2); }
    SGB(0x8, 2);
    BARRIER();
    {
      v4u x0 = f1.x[0];
      if (t == 0) x0 = ones;
      aq[0] = mfma32(f1.w[0], x0, aq[0]); ak[0] = mfma32(f1.w[1], x0, ak[0]); av[0] = mfma32(x0, f1.w[2], av[0]);
#pragma unroll
      for (int i = 1; i < 5; ++i) { ak[i] = mfma32(f1.w[1], f1.x[i], ak[i]); aq[i] = mfma32(f1.w[0], f1.x[i], aq[i]); }
#pragma unroll
      for (int i = 1; i < 5; ++i) av[i] = mfma32(f1.x[i], f1.w[2], av[i]);
    }
    STAMP(2); CSTAMP(25);
    v2u kb[5], vb[5], qb[5];
    f32x4 csq, csk;
#pragma unroll
    for (int r = 0; r < 4; ++r) { csq[r] = dppf<0x150>(aq[0][r]); csk[r] = dppf<0x150>(ak[0][r]); }
    const float csv = __shfl(av[0][0], t);
#pragma unroll
    for (int i = 0; i < 5; ++i) {
      kb[i] = pk4(ak[i] * rb1[i][0] + csk * rb1[i][1]);
      if (i > 0) qb[i] = pk4(aq[i] * rb1[i][0] + csq * rb1[i][1]);
    }
    SB0();
    f32x4 sp[4], sc[4];
#pragma unroll
    for (int i = 0; i < 4; ++i) { sp[i] = mfma16(kb[i], qb[i + 1], zero4); sc[i] = mfma16(kb[i + 1], qb[i + 1], zero4); }
#pragma unroll
    for (int i = 0; i < 5; ++i)
      vb[i] = v2u{pk2(av[i][0] * rbA[i][0] + csv * rbA[i][1], av[i][1] * rbA[i][2] + csv * rbA[i][3]),
                  pk2(av[i][2] * rbB[i][0] + csv * rbB[i][1], av[i][3] * rbB[i][2] + csv * rbB[i][3])};
    SB0();
    const bool par = t & 1;
    const bool cur0 = 4 * G + (t & 1) <= t, cur1 = 4 * G + (t & 1) + 2 <= t;
    const u32 sh = (t & 1) * 16;
    float z0[4], z1[4], m[4];
#pragma unroll
    for (int i = 0; i < 4; ++i) {
      const float c0 = par ? sc[i][1] : sc[i][0], c1 = par ? sc[i][3] : sc[i][2];
      float p0 = par ? sp[i][1] : sp[i][0], p1 = par ? sp[i][3] : sp[i][2];
      if (i == 0 && g == 0) { p0 = -4e9f; p1 = -4e9f; }
      z0[i] = cur0 ? c0 : p0; z1[i] = cur1 ? c1 : p1;
      m[i] = vmax(z0[i], z1[i]);
    }
    SB0();
#pragma unroll
    for (int i = 0; i < 4; ++i) { auto s_ = __builtin_amdgcn_permlane16_swap(asu(m[i]), asu(m[i]), false, false); m[i] = vmax(asf(s_[0]), asf(s_[1])); }
    SB0();
#pragma unroll
    for (int i = 0; i < 4; ++i) { auto s_ = __builtin_amdgcn_permlane32_swap(asu(m[i]), asu(m[i]), false, false); m[i] = vmax(asf(s_[0]), asf(s_[1])); }
    SB0();
    f32x4 o[4], den[4];
#pragma unroll
    for (int i = 0; i < 4; ++i) {
      const float e0 = __builtin_amdgcn_exp2f((z0[i] - m[i]) * 0.36067376022f), e1 = __builtin_amdgcn_exp2f((z1[i] - m[i]) * 0.36067376022f);
      const u32 u = pk2(e0, e1);
      const u32 w0 = (u & 0xffffu) << sh, w1_ = (u >> 16) << sh;
      const v4u P = {cur0 ? 0u : w0, cur1 ? 0u : w1_, cur0 ? w0 : 0u, cur1 ? w1_ : 0u};
      const v4u V = {vb[i].x, vb[i].y, vb[i + 1].x, vb[i + 1].y};
      den[i] = mfma32(ones, P, zero4);
      o[i] = mfma32(V, P, zero4);
    }
    SB0();
#pragma unroll
    for (int i = 0; i < 4; ++i) {
      const v2u ob = pk4(o[i] * __builtin_amdgcn_rcpf(den[i][0]));
      __builtin_amdgcn_raw_buffer_store_b64(ob, rws, lane * 8, G_O + (i * 32 + 4 * j + lo) * 512, 16);
    }
    SB0();
    STAMP(23);
    woread();
    gflag_pub(gw, G_FLA + (j * 4 + lo) * FSP, tag, lane);
    STAMP(3);
    __builtin_amdgcn_s_setprio(0);
    w2cell(0, L_W2 + lo * 4096);
#pragma unroll
    for (int r = 0; r < 18; ++r) wr[r] = v4u{0, 0, 0, 0};
  } else {
    __builtin_amdgcn_s_setprio(3);
    auto item = [&](auto I) {
      constexpr int it = decltype(I)::value;
      if constexpr (it >= 30) w1load(it - 30, (it - 30 + 4) % 6);
      else if constexpr (it >= 24) { wcell_dma_half(p.wo + (size_t)(4 * lo + it - 24) * 32768, wev, (it - 24) * 16384 + lo * 4096, 0); wcell_dma_half(p.wo + (size_t)(4 * lo + it - 24) * 32768, wev, (it - 24) * 16384 + lo * 4096, 1); }
      else if constexpr (it >= 16) xload((it - 16) / 2, it & 1);
      else if constexpr (it >= 0) wload(it);
    };
    const u32 xo0 = L_X + lo * 2048 + xl;
    const int t4 = 4 * lo + (t & 3);
    const u32 x4 = L_X + ((t >> 3) * 5 + 4) * 2048 + (t4 >> 3) * 1024 + (t4 & 7) * 128 + ((((t >> 2) & 1) * 4 + G) << 4);
    f32x4 s0 = zero4, q0 = zero4, s4 = zero4, q4 = zero4;
    v4u xs[2][3];
    float sq4 = 0.f;
    auto diag = [&](f32x4 v) { const float l_ = (t & 1) ? v[1] : v[0], h_ = (t & 1) ? v[3] : v[2]; return (t & 2) ? h_ : l_; };
    auto step = [&](auto I) {
      constexpr int be = decltype(I)::value;
      vmwait<issued_before_barrier(be) - need_before_barrier(be)>();
      if (P1ONLY) STAMP(12 + be);
      BARRIER();
      if constexpr (be >= 4 && be <= 10) {
#pragma unroll
        for (int k = 0; k < 3; ++k) {
          const int i = 3 * (be - 4) + k, P = i / 5, kk = i % 5;
          if (i < 20) xs[be & 1][k] = kk < 4 ? lds128((xo0 ^ ((kk & 1) * 64)) + (2 * P + (kk >> 1)) * 10240) : lds128(x4 + P * 20480);
        }
      }
      if constexpr (be == 10) { xres[0] = lds64(xra); xres[1] = lds64(xra ^ 32); }
      SB0();
      item(IC(SCH[be + 1][0])); item(IC(SCH[be + 1][1]));
      SB0();
      if constexpr (be >= 5 && be <= 11) {
#pragma unroll
        for (int k = 0; k < 3; ++k) {
          constexpr int i0 = 3 * (be - 5);
          if (i0 + k < 20) {
            const v4u xx = xs[(be - 1) & 1][k];
            if ((i0 + k) % 5 < 4) { s0 = mfma32(ones, xx, s0); q0 = mfma32(xx, xx, q0); }
            else { s4 = mfma32(ones, xx, s4); q4 = mfma32(xx, xx, q4); }
          }
        }
      }
      if constexpr (be == 2) { asm volatile("" : "+v"(epv)); tag = mix64(((u64)uni((u32)(epv >> 32)) << 32) | uni((u32)epv)); }
      if constexpr (be == 12) {
        float r, bb;
        ln_coef(s0[0], diag(q0), r, bb);
        if (G == (t >> 2)) *(LAS f32x2*)(C_R + (16 * lo + t) * 8) = f32x2{r, bb};
        sq4 = xg_sum(G == (t >> 2) ? diag(q4) : 0.f);
      }
      if constexpr (be == 13) {
        float r2, b2, sm = s4[0], sq = sq4;
        sm += dppf<0x128>(sm); sq += dppf<0x128>(sq); sm += dppf<0x124>(sm); sq += dppf<0x124>(sq);
        ln_coef(sm, sq, r2, b2);
        if (lane < 4) *(LAS f32x2*)(C_R + (64 + t4) * 8) = f32x2{r2, b2};
      }
    };
    UNROLL_SEQ(step, 16);
    vmwait<6>();
    __builtin_amdgcn_s_setprio(1);
    BARRIER();
    STAMP(2);
#pragma unroll
    for (int ks = 2; ks < 6; ++ks) w1load(ks, (ks + 4) % 6);
    woread();
    VMWAIT0();
    lflag_set(FL_W1L + lo, 1);
    lwait4(FL_W1L, 1);
#pragma unroll
    for (int r = 0; r < 6; ++r) w1read(r, (r + 4) % 6);
    lflag_set(FL_W1C + lo, 1);
    lwait4(FL_W1C, 1);
    STAMP(23);
#pragma unroll
    for (int ks = 6; ks < 12; ++ks) w1load(ks, ks - 6);
    STAMP(3);
    __builtin_amdgcn_s_setprio(0);
  }
  if (P1ONLY) { VMWAIT0(); STAMP(8); STAMP_OUT(); return; }
  v4u hres;
  {
    if (hi == 0) poll8(gw, G_FLA + lo * 8 * FSP, tag, lane); else VMWAIT0();
    BARRIER();
    STAMP(4);
    const u32 c0 = hi * 32768 + lo * 4096;
#pragma unroll
    for (int i = 0; i < 2; ++i) dma4_sc1(gw + G_O + c0 + i * 16384, lane * 16, c0 + i * 16384);
    if (hi == 0) { w2read(0, L_W2 + lo * 4096); asm volatile("; w2 0" : "+v"(wr[3])); }
    v4u bf[2][4];
    f32x4 acc[2][4];
    u32 oa = c0 + lane * 8, ob = oa + 512;
    asm volatile("" : "+v"(oa), "+v"(ob));
    vmwait<4>();
    STAMP(9);
#pragma unroll
    for (int s = 0; s < 4; ++s) bf[0][s] = act_frag(oa, ob, s * 1024);
    SB0();
#pragma unroll
    for (int s = 0; s < 2; ++s)
#pragma unroll
      for (int c = 0; c < 4; ++c) acc[0][c] = mfma32(wo[s].f[c], bf[0][s], s == 0 ? zero4 : acc[0][c]);
    SB0();
    VMWAIT0();
#pragma unroll
    for (int s = 0; s < 4; ++s) bf[1][s] = act_frag(oa, ob, 16384 + s * 1024);
    SB0();
#pragma unroll
    for (int s = 2; s < 4; ++s)
#pragma unroll
      for (int c = 0; c < 4; ++c) acc[0][c] = mfma32(wo[s].f[c], bf[0][s], acc[0][c]);
    SB0();
#pragma unroll
    for (int s = 0; s < 4; ++s)
#pragma unroll
      for (int c = 0; c < 4; ++c) acc[1][c] = mfma32(wo[s].f[c], bf[1][s], s == 0 ? zero4 : acc[1][c]);
#pragma unroll
    for (int c = 0; c < 4; ++c) *(LAS f32x4*)(c0 + c * 1024 + lane * 16) = acc[0][c];
    SB0();
#pragma unroll
    for (int c = 0; c < 4; ++c) *(LAS f32x4*)(c0 + 16384 + c * 1024 + lane * 16) = acc[1][c];
    if (hi == 0) w2cell(1, L_W2 + lo * 4096);
    STAMP(16);
    BARRIER();
    STAMP(17);
    const u32 pb = lo * 16384 + hi * 2048 + lane * 16;
    f32x4 hv[2];
#pragma unroll
    for (int c = 0; c < 2; ++c)
      hv[c] = ((ldsf4(pb + c * 1024) + ldsf4(pb + 4096 + c * 1024)) + (ldsf4(pb + 8192 + c * 1024) + ldsf4(pb + 12288 + c * 1024))) + unpk4(xres[c].x, xres[c].y);
    const v2u h0 = pk4(hv[0]), h1 = pk4(hv[1]);
    hres = v4u{h0.x, h0.y, h1.x, h1.y};
    __builtin_amdgcn_raw_buffer_store_b128(hres, rws, lane * 16, G_H + (lo * 16 + 2 * j + hi) * 1024, 16);
    SB0();
    const f32x4 s4 = hv[0] + hv[1], q4 = hv[0] * hv[0] + hv[1] * hv[1];
    const float sm = xg_sum((s4[0] + s4[1]) + (s4[2] + s4[3])), sq = xg_sum((q4[0] + q4[1]) + (q4[2] + q4[3]));
    if (lane < 16) __builtin_amdgcn_raw_buffer_store_b64(v2u{asu(sm), asu(sq)}, rws, lane * 8, G_ST + (j >> 1) * 2048 + lo * 512 + ((j & 1) * 2 + hi) * 128, 16);
    SB0();
  }
  STAMP(20);
  const u32 fbo = G_FLB + ((lo & 1) * 32 + j * 4 + (lo >> 1) * 2 + hi) * FSP;
  f32x4 cs[3] = {zero4, zero4, zero4};
  if (hi) {
    *(LAS v4u*)(C_HR + lo * 1024 + lane * 16) = hres;
#pragma unroll
    for (int r = 6; r < 12; ++r) w1read(r, r - 6);
  }
  gflag_pub(gw, fbo, tag, lane);
  STAMP(5);
  if (hi == 0) { w2read(1, L_W2 + lo * 4096); asm volatile("; w2 1" : "+v"(wr[7])); }
  BARRIER();
  w1load(12 + 2 * hi, 2 * hi); w1load(13 + 2 * hi, 2 * hi + 1);
  if (hi == 0) { w2cell(2, L_W2 + lo * 4096); w2cell(3, L_RING + 4 * SLOT + lo * 4096); }
  if (hi) {
#pragma unroll
    for (int r = 0; r < 12; ++r)
#pragma unroll
      for (int u = 0; u < 3; ++u) cs[u] = mfma32(wr[3 * r + u], ones, cs[u]);
  }
  poll8(gw, G_FLB + (hi * 32 + lo * 8) * FSP, tag, lane);
  STAMP(6);
  dma4_sc1(gw + G_H + (hi * 16 + 4 * lo) * 1024, lane * 16, hi * 16384 + lo * 4096);
  dma4_sc1(gw + G_H + ((hi + 2) * 16 + 4 * lo) * 1024, lane * 16, (hi + 2) * 16384 + lo * 4096);
  BARRIER();
  dma1_sc1(gw + G_ST + w * 1024, lane * 16, C_S2 + w * 1024);
  int tz = tid;
  auto pollf = [&](int i) {
    const u64 v = __hip_atomic_load((GAS u64*)(gw + G_FLC + lo * 8 * FSP + i * FBL + (u32)(tz & 7) * FSP), __ATOMIC_RELAXED, __HIP_MEMORY_SCOPE_AGENT);
    const bool r = (__builtin_amdgcn_ballot_w64(v == tag) & 0xff) == 0xff; MEMFENCE(); return r;
  };
  auto waitf = [&](int i) {
#pragma unroll 1
    for (u32 s = 0; s < SPIN_G && !pollf(i); ++s) {}
  };
  auto fslot = [&](int i) -> u32 { return i == 2 ? lo * FPART : L_W2 + lo * AWP + (i == 1 ? FPART : 0); };
  auto fload = [&](int i) {
    const char* src = gw + G_F + i * F_TAU + lo * FPART;
    const u32 dst = fslot(i);
    const u32 l16 = (tz & 63) * 16;
    dma8_sc1(src + 4096, l16, dst + 4096); dma2_sc1(src + 8192, l16, dst + 8192);
    if (lo < 3) dma_more2048_sc1(src + 8192, l16);
  };
  char* const op = p.out + (size_t)T0 * 1024 + j * 128;
  auto fin = [&](int i, u32 buf, u32 pitch, int tok) {
    const int lane = tz & 63;
    const u32 pr = buf + (tok + (lane >> 5)) * 272 + (lane & 31) * 8;
    const f32x2 sv = (ldsf2(pr) + ldsf2(pr + pitch)) + (ldsf2(pr + 2 * pitch) + ldsf2(pr + 3 * pitch));
    *(u32*)(op + (u32)((16 * i + tok + (lane >> 5)) * 1024 + (lane & 31) * 4)) = pk2(sv[0], sv[1]);
  };
  asm volatile("" : "+s"(hr_));

  if (hr_ == 0) {
    asm volatile("" : "+v"(tz));
    const int lane = tz & 63, t = lane & 15, G = lane >> 4;
    const u32 wev = wcell_voff(lane, j), ro = wcell_roff(lane);
    u32 rw = ro;
    asm volatile("" : "+v"(rw));
    auto w2rd = [&](int s, u32 cell) {
      const u32 ad = cell + rw;
#pragma unroll
      for (int k = 0; k < 4; ++k) wr[4 * s + k] = trfrag(ad ^ (k * 32), 512);
    };
    w2rd(2, L_W2 + lo * 4096); w2rd(3, L_RING + 4 * SLOT + lo * 4096);
    vmwait<5>();
    BARRIER();
    STAMP(7);
    const u32 aw = L_W2 + lo * AWW;
    auto w2c = [&](int s) { const char* src = w2s + ((s == 10 && lo == 3) ? 9 : s) * 32768; wcell_dma_half(src, wev, aw + (s % 5) * 4096, 0); wcell_dma_half(src, wev, aw + (s % 5) * 4096, 1); };
    w2c(4); w2c(5);
    vmwait<8>();
    BARRIER();
    {
      const u32 sta = C_S2 + G * 2048 + lo * 512 + t * 8;
      const f32x2 a_ = (ldsf2(sta) + ldsf2(sta + 128)) + (ldsf2(sta + 256) + ldsf2(sta + 384));
      const float sm = xg_sum(a_[0]);
      float r, bb; ln_coef(sm, xg_sum(a_[1]), r, bb);
      if (SCALEP) bb = sm * (-1.0f / 512.0f);
      if (lane < 16) *(LAS f32x2*)(C_TB + (lo * 16 + t) * 8) = f32x2{r, bb};
      lflag_set(FL_COEF + lo, 1);
    }
    w2c(6); w2c(7); w2c(8);
    vmwait<12>();
    w2rd(4, aw + 4 * 4096); w2rd(5, aw);
    LGWAIT0();
    w2c(9); w2c(10);
    vmwait<8>();
    w2rd(6, aw + 4096); w2rd(7, aw + 2 * 4096); w2rd(8, aw + 3 * 4096);
    VMWAIT0();
    w2rd(9, aw + 4 * 4096); w2rd(10, aw);
    const v4u hre2 = lds128(C_HR + lo * 1024 + lane * 16);
    lflag_set(FL_W2D + lo, 1);
    STAMP(14);
    lwait4(FL_W2D, 1);
    waitf(0);
    STAMP(19);
    fload(0);
    __builtin_amdgcn_s_setprio(2);
    const u32 pw = t * 272 + G * 16;
    f32x4 acc[4];
    auto init = [&](int i) {
      const u32 mk = (i == lo) ? 0xffffffffu : 0u;
      acc[0] = unpk4(hres.x & mk, hres.y & mk); acc[1] = unpk4(hres.z & mk, hres.w & mk);
      acc[2] = unpk4(hre2.x & mk, hre2.y & mk); acc[3] = unpk4(hre2.z & mk, hre2.w & mk);
      asm volatile("" : "+v"(acc[0]), "+v"(acc[1]), "+v"(acc[2]), "+v"(acc[3]));
    };
    auto tile = [&](int i, u32 buf) {
      const u32 sa = fslot(i) + lane * 8;
      constexpr int PD = 4;
      v4u bf[11];
      auto rd = [&](int k) { bf[k] = act_frag(sa, sa + 512, k * 1024); SB0(); };
      SB0();
#pragma unroll
      for (int k = 0; k < PD; ++k) rd(k);
#pragma unroll
      for (int k = 0; k < 10; ++k) {
        acc[0] = mfma32(wr[4 * k], bf[k], acc[0]);
        SB0();
        if (k + PD < 11) rd(k + PD);
#pragma unroll
        for (int c = 1; c < 4; ++c) acc[c] = mfma32(wr[4 * k + c], bf[k], acc[c]);
        SB0();
      }
      if (lo < 3) {
#pragma unroll
        for (int c = 0; c < 4; ++c) acc[c] = mfma32(wr[40 + c], bf[10], acc[c]);
      }
      MEMFENCE();
#pragma unroll
      for (int c = 0; c < 4; ++c) *(LAS f32x4*)(buf + pw + c * 64) = acc[c];
    };
    init(0);
    lwait4(FL_BDONE, 4);
    VMWAIT0();
    STAMP(12);
#pragma unroll 1
    for (int i = 0; i < 4; ++i) {
      if (i == 3) { STAMP(13); }
      tile(i, i == 0 ? L_P4 + lo * PQ : fslot(i));
      if (i == 0) lflag_set(FL_A0 + lo, 1);
      SB0();
      init(i + 1);
      if (i == 2) BARRIER();
      else if (i == 1) lwait1(FL_FRDY + lo, 2);
      else if (i == 0) lwait1(FL_FRDY + lo, 1);
    }
    BARRIER();
    fin(3, L_W2, AWP, 2 * w);
    if (w == 0 && j == 0 && lane == 0) __hip_atomic_store((GAS u64*)(gw + G_EPOCH), tag, __ATOMIC_RELAXED, __HIP_MEMORY_SCOPE_AGENT);
  } else {
#pragma unroll
    for (int r = 12; r < 16; ++r) w1read(r, r - 12);
    SB0();
#pragma unroll
    for (int r = 12; r < 16; ++r)
#pragma unroll
      for (int u = 0; u < 3; ++u) cs[u] = mfma32(wr[3 * r + u], ones, cs[u]);
    if (t == 0) {
#pragma unroll
      for (int u = 0; u < 3; ++u) *(LAS f32x4*)(C_CS1 + (lo * 3 + u) * 64 + G * 16) = cs[u];
    }
    __builtin_amdgcn_s_setprio(3);
    vmwait<5>();
    BARRIER();
    STAMP(7); CSTAMP(29);
    const u32 fco = G_FLC + (j * 4 + lo) * FSP;
    int pend = 0;
    auto pubto = [&](int stored) {
      VMWAIT0();
      if (lane < stored - pend) __hip_atomic_store((GAS u64*)(gw + fco + (pend + lane) * FBL), tag, __ATOMIC_RELAXED, __HIP_MEMORY_SCOPE_AGENT);
      MEMFENCE();
    };
    {
      constexpr int PD = 3;
      v4u hf[64 + PD];
      f32x4 acc[4][3], c4;
      f32x2 rb;
      u32 csa = C_CS1 + lo * 192 + G * 16, tba = C_TB + t * 8;
      asm volatile("" : "+v"(csa), "+v"(tba));
      const u32 hl = lane * 16;
      auto epi = [&](int i, int u) {
        f32x4 v = acc[i][u] * rb[0] + c4 * rb[1];
        const v2u fo = {pk2(fmaxf(v[0], 0.f), fmaxf(v[1], 0.f)), pk2(fmaxf(v[2], 0.f), fmaxf(v[3], 0.f))};
        if (u < 2 || nv == 3) __builtin_amdgcn_raw_buffer_store_b64(fo, rws, lane * 8, G_F + i * F_TAU + (Tj + lt + u) * 512, 16);
      };
#pragma unroll
      for (int k = 0; k < PD; ++k) hf[k] = lds128(hl + k * 1024);
      auto p3step = [&](auto I) {
        constexpr int n = decltype(I)::value, i = n >> 4, ks = n & 15;
        if (ks == 0) mfma32z(acc[i][0], wr[3 * ks], hf[n]); else mfma32t(acc[i][0], wr[3 * ks], hf[n]);
        SB0();
        if constexpr (n == 9) { VMWAIT0(); BARRIER(); }
        if constexpr (n == 15) lwait4(FL_COEF, 1);
        if constexpr (n + PD < 64) hf[n + PD] = lds128(hl + (n + PD) * 1024);
        if constexpr (ks == 15) rb = ldsf2(tba + i * 128);
        if constexpr (i > 0) {
          if constexpr ((ks == 10 && (VIDLE & 1)) || (ks == 13 && (VIDLE & 2))) { const bool go = pend < i && vm_idle(); if (go) pubto(i); pend = go ? i : pend; }
        }
        SB0();
        if (ks == 0) mfma32z(acc[i][1], wr[3 * ks + 1], hf[n]); else mfma32t(acc[i][1], wr[3 * ks + 1], hf[n]);
        SB0();
        if constexpr (i > 1 && ks == 1) { if (pend < i - 1) pubto(i - 1); pend = i - 1; }
        if constexpr (i > 0 && ks >= 1 && ks <= 3) {
          if constexpr (ks == 1) asm volatile("s_nop 2" ::: "memory");
          epi(i - 1, ks - 1);
        }
        if constexpr (i > 0 && ks <= 2) c4 = ldsf4(csa + ks * 64);
        SB0();
        if (ks == 0) mfma32z(acc[i][2], wr[3 * ks + 2], hf[n]); else mfma32t(acc[i][2], wr[3 * ks + 2], hf[n]);
        SB0();
        if constexpr (ks == 15) { STAMP(i < 3 ? 26 + i : 22); }
      };
      UNROLL_SEQ(p3step, 64);
      lflag_set(FL_BDONE + lo, 4);
      f32x4 cl[3];
#pragma unroll
      for (int u = 0; u < 3; ++u) cl[u] = ldsf4(csa + u * 64);
      if (pend < 3) pubto(3);
      pend = 3;
      asm volatile("s_nop 7" : "+v"(acc[3][0]), "+v"(acc[3][1]), "+v"(acc[3][2]));
#pragma unroll
      for (int u = 0; u < 3; ++u) { c4 = cl[u]; epi(3, u); }
    }
    STAMP(18);
    asm volatile("" : "+v"(tz));
    bool g1 = pollf(1);
    pubto(4);
    STAMP(19);
    __builtin_amdgcn_s_setprio(1);
    lwait4(FL_W2D, 1);
    if (!g1) waitf(1);
    fload(1);
    bool g2 = pollf(2);
    VMWAIT0(); lflag_set(FL_FRDY + lo, 1);
    if (!g2) waitf(2);
    lwait4(FL_BDONE, 4);
    fload(2);
    bool g3 = pollf(3);
    VMWAIT0(); lflag_set(FL_FRDY + lo, 2);
    if (!g3) waitf(3);
    lwait1(FL_A0 + lo, 1);
    STAMP(15);
    fload(3);
    VMWAIT0(); lflag_set(FL_FRDY + lo, 3);
    STAMP(21);
    BARRIER();
#pragma unroll
    for (int k = 0; k < 2; ++k) { fin(0, L_P4, PQ, 4 * lo + 2 * k); fin(1, L_W2 + FPART, AWP, 4 * lo + 2 * k); fin(2, 0, FPART, 4 * lo + 2 * k); }
    BARRIER();
    fin(3, L_W2, AWP, 2 * w);
  }
  STAMP(8); CSTAMP(11);
  STAMP_OUT();
}
}

extern "C" void kernel_launch(void* const* d_in, const int* in_sizes, int n_in, void* d_out, int out_size, void* d_ws, size_t ws_size,
                              hipStream_t stream) {
  fb::Params p;
  p.x = (const char*)d_in[0]; p.wqkv = (const char*)d_in[1]; p.wo = (const char*)d_in[2];
  p.w1 = (const char*)d_in[4]; p.w2 = (const char*)d_in[5];
  p.out = (char*)d_out; p.ws = (char*)d_ws;
  (void)hipFuncSetAttribute((const void*)fb::fused_block, hipFuncAttributeMaxDynamicSharedMemorySize, fb::L_TOTAL);
  fb::fused_block<<<256, 512, fb::L_TOTAL, stream>>>(p);
}
```

```cpp
#include <hip/hip_runtime.h>
#include <hip/hip_bf16.h>
#include <utility>
#pragma clang diagnostic ignored "-Wint-to-pointer-cast"
#pragma clang diagnostic ignored "-Wc++20-extensions"
namespace fb {
typedef unsigned u32; typedef unsigned long long u64; typedef unsigned short u16;
typedef u32 v4u __attribute__((ext_vector_type(4)));
typedef u32 v2u __attribute__((ext_vector_type(2)));
typedef float f32x4 __attribute__((ext_vector_type(4)));
typedef float f32x2 __attribute__((ext_vector_type(2)));
typedef __bf16 bf8 __attribute__((ext_vector_type(8)));
typedef __bf16 bf2 __attribute__((ext_vector_type(2)));
typedef short s4 __attribute__((ext_vector_type(4)));
#define LAS __attribute__((address_space(3)))
#define GAS __attribute__((address_space(1)))
#define DEV __device__ __forceinline__
#define IC(x) std::integral_constant<int, (x)>{}
#define UNROLL_SEQ(fn, N) [&]<int... Is_>(std::integer_sequence<int, Is_...>) { (fn(std::integral_constant<int, Is_>{}), ...); }(std::make_integer_sequence<int, N>{})

constexpr u32 G_EPOCH = 0;
constexpr u32 G_O = 8192;
constexpr u32 G_H = G_O + 65536;
constexpr u32 G_F = G_H + 65536;
constexpr u32 F_TAU = 86 * 512;
constexpr u32 G_ST = 315392 + 1024;
constexpr u32 FSP = 256, FBL = 32 * FSP;
constexpr u32 G_FLA = 327680, G_FLB = G_FLA + 2 * FBL, G_FLC = G_FLB + 2 * FBL, G_BYTES = G_FLC + 4 * FBL;
static_assert(4 * G_BYTES <= (2u << 20) && G_ST + 8192 <= G_FLA, "workspace map");
__host__ __device__ constexpr size_t group_base(int g) { return (size_t)(g >> 2) * (2u << 20) + (size_t)(g & 3) * G_BYTES; }
constexpr u32 L_X = 0, L_RING = 81920, SLOT = 12288, L_CTRL = 155648, L_TOTAL = 163840;
constexpr u32 L_W2 = 65536, C_S2 = 147456;
constexpr u32 FPART = 11264, L_P4 = 45056, PQ = 4352;
constexpr u32 C_R = L_CTRL;
constexpr u32 C_CS1 = L_CTRL + 1024;
constexpr u32 C_FLAGS = L_CTRL + 768, FL_W1L = 0, FL_W1C = 4, FL_COEF = 8, FL_W2D = 12, FL_BDONE = 16, FL_FRDY = 20, FL_A0 = 24, NSLOT = 6;
constexpr u32 C_TB = L_CTRL + 2048, C_HR = L_CTRL + 4096, AWP = 22528, AWW = 20480;
constexpr u32 SPIN_G = 1u << 16, SPIN_L = 1u << 20;

struct Params { const char* x; const char* wqkv; const char* wo; const char* w1; const char* w2; char* out; char* ws; };

DEV f32x4 mfma32(v4u a, v4u b, f32x4 c) { return __builtin_amdgcn_mfma_f32_16x16x32_bf16(__builtin_bit_cast(bf8, a), __builtin_bit_cast(bf8, b), c, 0, 0, 0); }
DEV f32x4 mfma16(v2u a, v2u b, f32x4 c) { return __builtin_amdgcn_mfma_f32_16x16x16bf16_1k(__builtin_bit_cast(s4, a), __builtin_bit_cast(s4, b), c, 0, 0, 0); }
DEV void mfma32t(f32x4& c, v4u a, v4u b) { asm volatile("v_mfma_f32_16x16x32_bf16 %0, %1, %2, %0" : "+v"(c) : "v"(a), "v"(b)); }
DEV void mfma32z(f32x4& c, v4u a, v4u b) { asm volatile("v_mfma_f32_16x16x32_bf16 %0, %1, %2, 0" : "=&v"(c) : "v"(a), "v"(b)); }
DEV u32 pk2(float a, float b) { bf2 p = {(__bf16)a, (__bf16)b}; return __builtin_bit_cast(u32, p); }
DEV v2u pk4(f32x4 v) { return v2u{pk2(v[0], v[1]), pk2(v[2], v[3])}; }
DEV float asf(u32 u) { return __builtin_bit_cast(float, u); }
DEV u32 asu(float f) { return __builtin_bit_cast(u32, f); }
DEV f32x4 unpk4(u32 lo, u32 hi) { return f32x4{asf(lo << 16), asf(lo & 0xffff0000u), asf(hi << 16), asf(hi & 0xffff0000u)}; }
DEV u32 uni(u32 v) { return __builtin_amdgcn_readfirstlane(v); }
DEV v4u lds128(u32 a) { return *(const LAS v4u*)a; }
DEV v2u lds64(u32 a) { return *(const LAS v2u*)a; }
DEV f32x4 ldsf4(u32 a) { return *(const LAS f32x4*)a; }
DEV f32x2 ldsf2(u32 a) { return *(const LAS f32x2*)a; }
DEV v2u ldstr(u32 a) { return __builtin_bit_cast(v2u, __builtin_amdgcn_ds_read_tr16_b64_v4i16((LAS s4*)a)); }
DEV float vmax(float a, float b) { float r; asm("v_max_f32 %0, %1, %2" : "=v"(r) : "v"(a), "v"(b)); return r; }
#define MEMFENCE() asm volatile("" ::: "memory")
#define SB0() __builtin_amdgcn_sched_barrier(0)
#define BARRIER() do { SB0(); asm volatile("s_waitcnt lgkmcnt(0)\n\ts_barrier" ::: "memory"); SB0(); } while (0)
#define SGB(m, n) __builtin_amdgcn_sched_group_barrier(m, n, 0)
#define VMWAIT0() asm volatile("s_waitcnt vmcnt(0)" ::: "memory")
#define LGWAIT0() asm volatile("s_waitcnt lgkmcnt(0)" ::: "memory")
template <int N> DEV void vmwait() { asm volatile("s_waitcnt vmcnt(%0)" ::"i"(N) : "memory"); }
#define GLD "global_load_lds_dwordx4 "
DEV void dma3(const char* sb, u32 v, u32 dst) {
  asm volatile("s_mov_b32 m0, %2\n\ts_nop 0\n\t" GLD "%0, %1\n\t" GLD "%0, %1 offset:1024\n\t" GLD "%0, %1 offset:2048" ::"v"(v), "s"(sb), "s"(dst) : "memory");
}
DEV void dma2v(const char* sb, u32 v0, u32 v1, u32 dst) {
  asm volatile("s_mov_b32 m0, %3\n\ts_nop 0\n\t" GLD "%0, %2\n\t" GLD "%1, %2 offset:1024" ::"v"(v0), "v"(v1), "s"(sb), "s"(dst) : "memory");
}
DEV void dma3v(const char* sb, u32 v0, u32 v1, u32 v2, u32 dst) {
  asm volatile("s_mov_b32 m0, %4\n\ts_nop 0\n\t" GLD "%0, %3\n\t" GLD "%1, %3 offset:1024\n\t" GLD "%2, %3 offset:2048"
               ::"v"(v0), "v"(v1), "v"(v2), "s"(sb), "s"(dst) : "memory");
}
DEV void dma1_sc1(const char* sb, u32 v, u32 dst) {
  asm volatile("s_mov_b32 m0, %2\n\ts_nop 0\n\t" GLD "%0, %1 sc1" ::"v"(v), "s"(sb), "s"(dst) : "memory");
}
DEV void dma4_sc1(const char* sb, u32 v, u32 dst) {
  asm volatile("s_mov_b32 m0, %2\n\ts_nop 0\n\t" GLD "%0, %1 sc1\n\t" GLD "%0, %1 offset:1024 sc1\n\t" GLD "%0, %1 offset:2048 sc1\n\t"
               GLD "%0, %1 offset:3072 sc1" ::"v"(v), "s"(sb), "s"(dst) : "memory");
}
DEV void dma8_sc1(const char* sb, u32 v, u32 dst) {
  asm volatile("s_mov_b32 m0, %2\n\ts_nop 0\n\t" GLD "%0, %1 offset:-4096 sc1\n\t" GLD "%0, %1 offset:-3072 sc1\n\t" GLD "%0, %1 offset:-2048 sc1\n\t"
               GLD "%0, %1 offset:-1024 sc1\n\t" GLD "%0, %1 sc1\n\t" GLD "%0, %1 offset:1024 sc1\n\t" GLD "%0, %1 offset:2048 sc1\n\t"
               GLD "%0, %1 offset:3072 sc1" ::"v"(v), "s"(sb), "s"(dst) : "memory");
}
DEV void dma2_sc1(const char* sb, u32 v, u32 dst) {
  asm volatile("s_mov_b32 m0, %2\n\ts_nop 0\n\t" GLD "%0, %1 sc1\n\t" GLD "%0, %1 offset:1024 sc1" ::"v"(v), "s"(sb), "s"(dst) : "memory");
}
DEV void dma_more2048_sc1(const char* sb, u32 v) { asm volatile(GLD "%0, %1 offset:2048 sc1" ::"v"(v), "s"(sb) : "memory"); }
DEV void lflag_set(u32 word, u32 v) {
  MEMFENCE();
  __hip_atomic_store((LAS u32*)(C_FLAGS + word * 4), v, __ATOMIC_RELAXED, __HIP_MEMORY_SCOPE_WORKGROUP);
  MEMFENCE();
}
DEV u32 lflag_min4(u32 word) {
  v4u v;
  asm volatile("ds_read_b128 %0, %1\n\ts_waitcnt lgkmcnt(0)" : "=v"(v) : "v"(C_FLAGS + word * 4) : "memory");
  u32 m = min(min(v.x, v.y), min(v.z, v.w));
  return uni(m);
}
DEV u32 lflag_get(u32 word) {
  u32 v;
  asm volatile("ds_read_b32 %0, %1\n\ts_waitcnt lgkmcnt(0)" : "=v"(v) : "v"(C_FLAGS + word * 4) : "memory");
  return uni(v);
}
DEV void lwait4(u32 word, u32 want) {
#pragma unroll 1
  for (u32 s = 0; s < SPIN_L && lflag_min4(word) < want; ++s) {} }
DEV void lwait1(u32 word, u32 want) {
#pragma unroll 1
  for (u32 s = 0; s < SPIN_L && lflag_get(word) < want; ++s) {} }
DEV void gflag_pub(char* gw, u32 off, u64 tag, int lane) {
  VMWAIT0();
  if (lane == 0) __hip_atomic_store((GAS u64*)(gw + off), tag, __ATOMIC_RELAXED, __HIP_MEMORY_SCOPE_AGENT);
  MEMFENCE();
}
DEV u64 gflag_poll(char* gw, u32 off, u64 tag, int lane, int n) {
  u64 v = __hip_atomic_load((GAS u64*)(gw + off + (lane & 7) * FSP + ((lane & (n - 1)) >> 3) * (32 * FSP)), __ATOMIC_RELAXED, __HIP_MEMORY_SCOPE_AGENT);
  u64 m = __builtin_amdgcn_ballot_w64(v == tag);
  MEMFENCE();
  return m;
}
DEV u64 mix64(u64 z) {
  z += 0x9E3779B97F4A7C15ull; z = (z ^ (z >> 30)) * 0xBF58476D1CE4E5B9ull; z = (z ^ (z >> 27)) * 0x94D049BB133111EBull; return z ^ (z >> 31);
}
DEV float xg_sum(float v) {
  auto a = __builtin_amdgcn_permlane16_swap(asu(v), asu(v), false, false);
  v = asf(a[0]) + asf(a[1]);
  auto b = __builtin_amdgcn_permlane32_swap(asu(v), asu(v), false, false);
  return asf(b[0]) + asf(b[1]);
}
template <int CTRL> DEV float dppf(float v) { return asf(__builtin_amdgcn_update_dpp(0u, asu(v), CTRL, 0xf, 0xf, false)); }
DEV void ln_coef(float s, float q, float& r, float& b) {
  float m = s * (1.0f / 512.0f);
  float var = fmaxf(q * (1.0f / 512.0f) - m * m, 0.0f);
  r = __builtin_amdgcn_rsqf(var + 1e-5f);
  b = -m * r;
}


struct Frag4 { v4u f[4]; };
DEV void wcell_dma_half(const char* src, u32 ve, u32 cell, int h) { dma2v(src + h * 8192, ve, (ve ^ 64) + 3072, cell + h * 2048); }
DEV u32 wcell_voff(int lane, int j) {
  const int r = lane >> 3;
  return (16 * (r >> 2) + (r & 3)) * 1024 + j * 128 + ((((lane & 7) >> 1) ^ ((r >> 1) & 1)) * 32) + (lane & 1) * 16;
}
DEV u32 wcell_roff(int lane) {
  const int G = lane >> 4, q = (lane & 15) >> 2;
  return G * 1024 + q * 128 + ((((q >> 1) & 1) | ((G & 1) << 1)) * 32) + (lane & 3) * 8;
}
DEV v4u trfrag(u32 ad, u32 d2) { v2u lo = ldstr(ad), hi = ldstr(ad + d2); return v4u{lo.x, lo.y, hi.x, hi.y}; }
DEV v4u act_frag(u32 a, u32 a2, u32 off) { v2u lo = lds64(a + off), hi = lds64(a2 + off); return v4u{lo.x, lo.y, hi.x, hi.y}; }
DEV void touchp(const char* ad) { asm volatile("s_mov_b32 m0, %1\n\ts_nop 0\n\tglobal_load_lds_dword %0, off" ::"v"(ad), "s"(L_CTRL + 3072) : "memory"); }
DEV void touch64(const char* ad) { asm volatile("s_mov_b32 m0, %1\n\ts_nop 0\n\tglobal_load_lds_dword %0, off sc1" ::"v"(ad), "s"(L_CTRL + 3072) : "memory"); }
DEV void poll8(char* gw, u32 off, u64 tag, int lane) {
#pragma unroll 1
  for (u32 s = 0; s < SPIN_G && (gflag_poll(gw, off, tag, lane, 8) & 0xff) != 0xff; ++s) {}
  VMWAIT0();
}

constexpr int SCH[17][4] = {{1, 2, 3, 4},     {5, 18, -1, -1},  {6, 19, -1, -1},  {7, 20, -1, -1},  {8, 21, -1, -1},  {9, 22, -1, -1},
                            {10, 23, -1, -1}, {11, -1, -1, -1}, {12, -1, -1, -1}, {13, -1, -1, -1}, {14, -1, -1, -1}, {15, -1, -1, -1},
                            {24, -1, -1, -1}, {25, -1, -1, -1}, {26, -1, -1, -1}, {27, -1, -1, -1}, {30, 31, -1, -1}};
constexpr int item_ops(int it) { return it < 0 ? 0 : it < 16 ? 3 : it < 24 ? ((it & 1) ? 2 : 3) : it < 30 ? 4 : 3; }
constexpr int item_end(int it) { int n = 0; for (int gI = 0; gI < 17; ++gI) for (int k = 0; k < 4; ++k) { n += item_ops(SCH[gI][k]); if (SCH[gI][k] == it) return n; } return 0; }
constexpr int issued_before_barrier(int b) { int n = 0; for (int gI = 0; gI <= b; ++gI) for (int k = 0; k < 4; ++k) n += item_ops(SCH[gI][k]); return n; }
constexpr int xpair_at(int b) { return b < 4 ? 0 : b < 7 ? 1 : b < 9 ? 2 : 3; }
constexpr int need_before_barrier(int b) { return item_end(b) > item_end(17 + 2 * xpair_at(b)) ? item_end(b) : item_end(17 + 2 * xpair_at(b)); }

__global__ void __launch_bounds__(512, 2) fused_block(Params p) {
  extern __shared__ __attribute__((aligned(16))) char smem[];
  const int tid = threadIdx.x, lane = tid & 63;
  const int w = uni(tid >> 6), lo = w & 3, hi = w >> 2;
  const int bid = blockIdx.x, xcd = bid & 7, idx = bid >> 3;
  const int g = xcd * 4 + (idx >> 3), j = idx & 7;
  const int T0 = g * 64;
  const int t = lane & 15, G = lane >> 4, rho = lane >> 3;
  char* const gw = p.ws + group_base(g);
  const __amdgpu_buffer_rsrc_t rws = __builtin_amdgcn_make_buffer_rsrc((void*)gw, 0, G_BYTES, 0x00020000);
  const char* pc0;
  asm volatile("s_getpc_b64 %0" : "=s"(pc0));
  if (tid < 8) touchp(pc0 + (idx * 8 + tid) * 120);
  asm volatile("" ::"s"(smem));
  const u32 wv = (8 * lo + rho) * 3072 + j * 128 + (((((lane & 7) >> 1) ^ (((rho >> 1) & 1) | ((lo & 1) << 1))) * 32) + (lane & 1) * 16);
  auto wload = [&](int c) { dma3(p.wqkv + (size_t)((c + 2 * xcd) & 15) * (32 * 3072), wv, L_RING + (c % NSLOT) * SLOT + lo * 3072); };
  u32 zz = 0; asm volatile("" : "+v"(zz));
  u64 epv;
  asm volatile("global_load_dwordx2 %0, %1, %2 sc1" : "=v"(epv) : "v"(zz), "s"(gw + G_EPOCH) : "memory");
  if (hi == 0) wload(0); else { wload(1); wload(2); wload(3); wload(4); }
  u32 xv[5];
#pragma unroll
  for (int n = 0; n < 5; ++n) {
    const int i = (5 * lo + n) % 10, row = T0 - 16 + 8 * i + rho;
    xv[n] = max(row, 0) * 1024 + ((((lane & 7) ^ (rho >> 1)) << 4) ^ ((i & 1) * 64)) + 2048 - (n < 3 ? n : n - 3) * 1024;
  }
  auto xload = [&](int P, int part) {
    const char* src = p.x - 2048 + ((2 * P + (lo >> 1) + xcd) & 7) * 128;
    if (part == 0) dma3v(src, xv[0], xv[1], xv[2], L_X + P * 20480 + 5 * lo * 1024);
    else dma2v(src, xv[3], xv[4], L_X + P * 20480 + (5 * lo + 3) * 1024);
  };
  if (hi == 0) { xload(0, 0); xload(0, 1); }
  const v4u ones = {0x3F803F80u, 0x3F803F80u, 0x3F803F80u, 0x3F803F80u};
  const f32x4 zero4 = {0, 0, 0, 0};
  const int Tj = 22 * (j >> 1) + ((j & 1) ? (j < 6 ? 12 : 10) : 0);
  const bool full = !(j & 1) && j < 6;
  const int lt = (full || lo < 3) ? 3 * lo : 8;
  const int nv = (full || lo < 2) ? 3 : 2;
  const u32 wev = wcell_voff(lane, j), ro = wcell_roff(lane);
  u32 w1v[3], ra[3];
  {
    const int r8 = (lane >> 2) & 7, mis = (r8 + (Tj >> 1)) & 1;
#pragma unroll
    for (int n = 0; n < 3; ++n) {
      int q6 = (2 * n + (lane >> 5) + 6 - mis) % 6;
      if (!full && q6 == 5) q6 = mis ? 0 : 4;
      w1v[n] = (16 * (r8 >> 2) + (r8 & 3) + 4 * lo) * 2752 + (Tj + 2 * q6 + (((lane >> 1) ^ lo) & 1)) * 32 + (lane & 1) * 16 + 2048 - n * 1024;
      ra[n] = L_RING + G * 3072 + ((((lt + n) >> 1) + mis) % 6) * 512 + ((lane & 15) >> 2) * 64 + ((((lt + n) ^ G) & 1) * 32) + (lane & 3) * 8;
      asm volatile("" : "+v"(ra[n]));
    }
  }
  auto w1load = [&](int ks, int sl) { dma3v(p.w1 - 2048 + (size_t)(32 * ks) * 2752, w1v[0], w1v[1], w1v[2], L_RING + sl * SLOT + lo * 3072); };
  v4u wr[48];
  auto w1read = [&](int r, int sl) {
#pragma unroll
    for (int u = 0; u < 3; ++u) wr[3 * r + u] = trfrag(ra[u] + sl * SLOT, 256);
  };
  const char* w2s = p.w2 + (size_t)(11 * lo) * 32768;
  auto w2cell = [&](int s, u32 cell) { wcell_dma_half(w2s + s * 32768, wev, cell, 0); wcell_dma_half(w2s + s * 32768, wev, cell, 1); };
  auto w2read = [&](int s, u32 cell) {
#pragma unroll
    for (int k = 0; k < 4; ++k) wr[4 * s + k] = trfrag(cell + (ro ^ (k * 32)), 512);
  };
  Frag4 wo[4];
  auto woread = [&]() {
#pragma unroll
    for (int s = 0; s < 4; ++s)
#pragma unroll
      for (int c = 0; c < 4; ++c) wo[s].f[c] = trfrag(s * 16384 + lo * 4096 + (ro ^ (c * 32)), 512);
  };
  v2u xres[2];
  const u32 xra = L_X + (((j - xcd) & 7) * 5 + lo + 1) * 2048 + (t >> 3) * 1024 + (t & 7) * 128 + (((4 * hi + (G >> 1)) ^ ((t >> 1) & 7)) << 4) + (G & 1) * 8;
  const u32 xl = (t >> 3) * 1024 + (t & 7) * 128 + ((G ^ ((t >> 1) & 7)) << 4);
  u64 tag;
  int hr_ = hi;
  asm volatile("" : "+s"(hr_));

  if (hr_ == 0) {
    __builtin_amdgcn_s_setprio(2);
    if (w == 0 && lane < 32) *(LAS u32*)(C_FLAGS + lane * 4) = 0;
    f32x4 aq[5], ak[5], av[5];
#pragma unroll
    for (int i = 0; i < 5; ++i) { aq[i] = zero4; ak[i] = zero4; av[i] = zero4; asm volatile("" : "+v"(aq[i]), "+v"(ak[i]), "+v"(av[i])); }
    const int q = (lane & 15) >> 2;
    const u32 woff = L_RING + G * 3072 + q * 128 + ((lo ^ (((q >> 1) & 1) | ((G & 1) << 1))) * 32) + (lane & 3) * 8;
    struct Fr { v4u w[3]; v4u x[5]; };
    auto ldfr = [&](Fr& f, u32 wa, u32 xa) {
#pragma unroll
      for (int m = 0; m < 3; ++m) f.w[m] = trfrag(wa + m * 1024, 512);
#pragma unroll
      for (int i = 0; i < 5; ++i) f.x[i] = lds128(xa + i * 2048);
    };
    auto mma = [&](Fr& f) {
      v4u x0 = f.x[0];
      if (t == 0) x0 = ones;
#pragma unroll
      for (int i = 1; i < 5; ++i) { aq[i] = mfma32(f.w[0], f.x[i], aq[i]); ak[i] = mfma32(f.w[1], f.x[i], ak[i]); av[i] = mfma32(f.x[i], f.w[2], av[i]); }
      aq[0] = mfma32(f.w[0], x0, aq[0]); ak[0] = mfma32(f.w[1], x0, ak[0]); av[0] = mfma32(x0, f.w[2], av[0]);
    };
    auto pat = [&]() {
#pragma unroll
      for (int i = 0; i < 6; ++i) { SGB(0x8, 1); SGB(0x100, 2); }
      SGB(0x8, 9);
    };
    Fr f0, f1;
    u32 so = 0, xa = L_X + xl;
    VMWAIT0();
    BARRIER();
    ldfr(f0, woff, xa);
    asm volatile("" : "+v"(epv));
    tag = mix64(((u64)uni((u32)(epv >> 32)) << 32) | uni((u32)epv));
    SB0();
#pragma unroll 1
    for (int it = 0; it < 7; ++it) {
      so = (so == (NSLOT - 1) * SLOT) ? 0 : so + SLOT;
      BARRIER();
      ldfr(f1, woff + so, xa ^ 64);
      mma(f0);
      pat();
      so = (so == (NSLOT - 1) * SLOT) ? 0 : so + SLOT;
      xa += 10240;
      BARRIER();
      ldfr(f0, woff + so, xa);
      mma(f1);
      pat();
      SB0();
      if (it <= 4) { xres[0] = lds64(xra); xres[1] = lds64(xra ^ 32); }
      if (it == 2) {
        const u32 id = bid * 102 + min(lo * 26 + (lane < 26 ? lane : 25), 101);
        touch64(id < 4096 ? p.wo + id * 128 : id < 15104 ? p.w1 + (id - 4096) * 128 : p.w2 + (id - 15104) * 128);
      }
      if (it == 4) {
        const u32 id = idx * 60 + lo * 15 + min(lane, 14);
        const u32 jo = (3u - (u32)((size_t)p.wo >> 7)) & 7u, j2 = (3u - (u32)((size_t)p.w2 >> 7)) & 7u;
        if (lane < 15) touchp(id < 512 ? p.wo + (id * 1024 + jo * 128) : p.w2 + (min(id - 512, 1375u) * 1024 + j2 * 128));
      }
      if (it == 3) {
        if (lane < 8) touch64(gw + G_FLA + (lane * 32 + j * 4 + lo) * FSP);
      }
      if (it == 5) {
        if (lane < 8) __hip_atomic_store((GAS u64*)(gw + G_FLA + (lane * 32 + j * 4 + lo) * FSP), ~tag, __ATOMIC_RELAXED, __HIP_MEMORY_SCOPE_AGENT);
        MEMFENCE();
      }
    }
    BARRIER();
    ldfr(f1, woff + 3 * SLOT, xa ^ 64);
    f32x2 rb1[5]; f32x4 rbA[5], rbB[5];
    {
      u32 ca = C_R + t * 8, cb = C_R + G * 32;
      asm volatile("" : "+v"(ca), "+v"(cb));
#pragma unroll
      for (int i = 0; i < 5; ++i) { rb1[i] = ldsf2(ca + i * 128); rbA[i] = ldsf4(cb + i * 128); rbB[i] = ldsf4(cb + i * 128 + 16); }
    }
    mma(f0);
#pragma unroll
    for (int i = 0; i < 13; ++i) { SGB(0x8, 1); SGB(0x100, 2); }
    SGB(0x8, 2);
    BARRIER();
    {
      v4u x0 = f1.x[0];
      if (t == 0) x0 = ones;
      aq[0] = mfma32(f1.w[0], x0, aq[0]); ak[0] = mfma32(f1.w[1], x0, ak[0]); av[0] = mfma32(x0, f1.w[2], av[0]);
#pragma unroll
      for (int i = 1; i < 5; ++i) { ak[i] = mfma32(f1.w[1], f1.x[i], ak[i]); aq[i] = mfma32(f1.w[0], f1.x[i], aq[i]); }
#pragma unroll
      for (int i = 1; i < 5; ++i) av[i] = mfma32(f1.x[i], f1.w[2], av[i]);
    }
    v2u kb[5], vb[5], qb[5];
    f32x4 csq, csk;
#pragma unroll
    for (int r = 0; r < 4; ++r) { csq[r] = dppf<0x150>(aq[0][r]); csk[r] = dppf<0x150>(ak[0][r]); }
    const float csv = __shfl(av[0][0], t);
#pragma unroll
    for (int i = 0; i < 5; ++i) {
      kb[i] = pk4(ak[i] * rb1[i][0] + csk * rb1[i][1]);
      if (i > 0) qb[i] = pk4(aq[i] * rb1[i][0] + csq * rb1[i][1]);
    }
    SB0();
    f32x4 sp[4], sc[4];
#pragma unroll
    for (int i = 0; i < 4; ++i) { sp[i] = mfma16(kb[i], qb[i + 1], zero4); sc[i] = mfma16(kb[i + 1], qb[i + 1], zero4); }
#pragma unroll
    for (int i = 0; i < 5; ++i)
      vb[i] = v2u{pk2(av[i][0] * rbA[i][0] + csv * rbA[i][1], av[i][1] * rbA[i][2] + csv * rbA[i][3]),
                  pk2(av[i][2] * rbB[i][0] + csv * rbB[i][1], av[i][3] * rbB[i][2] + csv * rbB[i][3])};
    SB0();
    const bool par = t & 1;
    const bool cur0 = 4 * G + (t & 1) <= t, cur1 = 4 * G + (t & 1) + 2 <= t;
    const u32 sh = (t & 1) * 16;
    float z0[4], z1[4], m[4];
#pragma unroll
    for (int i = 0; i < 4; ++i) {
      const float c0 = par ? sc[i][1] : sc[i][0], c1 = par ? sc[i][3] : sc[i][2];
      float p0 = par ? sp[i][1] : sp[i][0], p1 = par ? sp[i][3] : sp[i][2];
      if (i == 0 && g == 0) { p0 = -4e9f; p1 = -4e9f; }
      z0[i] = cur0 ? c0 : p0; z1[i] = cur1 ? c1 : p1;
      m[i] = vmax(z0[i], z1[i]);
    }
    SB0();
#pragma unroll
    for (int i = 0; i < 4; ++i) { auto s_ = __builtin_amdgcn_permlane16_swap(asu(m[i]), asu(m[i]), false, false); m[i] = vmax(asf(s_[0]), asf(s_[1])); }
    SB0();
#pragma unroll
    for (int i = 0; i < 4; ++i) { auto s_ = __builtin_amdgcn_permlane32_swap(asu(m[i]), asu(m[i]), false, false); m[i] = vmax(asf(s_[0]), asf(s_[1])); }
    SB0();
    f32x4 o[4], den[4];
#pragma unroll
    for (int i = 0; i < 4; ++i) {
      const float e0 = __builtin_amdgcn_exp2f((z0[i] - m[i]) * 0.36067376022f), e1 = __builtin_amdgcn_exp2f((z1[i] - m[i]) * 0.36067376022f);
      const u32 u = pk2(e0, e1);
      const u32 w0 = (u & 0xffffu) << sh, w1_ = (u >> 16) << sh;
      const v4u P = {cur0 ? 0u : w0, cur1 ? 0u : w1_, cur0 ? w0 : 0u, cur1 ? w1_ : 0u};
      const v4u V = {vb[i].x, vb[i].y, vb[i + 1].x, vb[i + 1].y};
      den[i] = mfma32(ones, P, zero4);
      o[i] = mfma32(V, P, zero4);
    }
    SB0();
#pragma unroll
    for (int i = 0; i < 4; ++i) {
      const v2u ob = pk4(o[i] * __builtin_amdgcn_rcpf(den[i][0]));
      __builtin_amdgcn_raw_buffer_store_b64(ob, rws, lane * 8, G_O + (i * 32 + 4 * j + lo) * 512, 16);
    }
    SB0();
    woread();
    gflag_pub(gw, G_FLA + (j * 4 + lo) * FSP, tag, lane);
    __builtin_amdgcn_s_setprio(0);
    w2cell(0, L_W2 + lo * 4096);
#pragma unroll
    for (int r = 0; r < 18; ++r) wr[r] = v4u{0, 0, 0, 0};
  } else {
    __builtin_amdgcn_s_setprio(3);
    auto item = [&](auto I) {
      constexpr int it = decltype(I)::value;
      if constexpr (it >= 30) w1load(it - 30, (it - 30 + 4) % 6);
      else if constexpr (it >= 24) { wcell_dma_half(p.wo + (size_t)(4 * lo + it - 24) * 32768, wev, (it - 24) * 16384 + lo * 4096, 0); wcell_dma_half(p.wo + (size_t)(4 * lo + it - 24) * 32768, wev, (it - 24) * 16384 + lo * 4096, 1); }
      else if constexpr (it >= 16) xload((it - 16) / 2, it & 1);
      else if constexpr (it >= 0) wload(it);
    };
    const u32 xo0 = L_X + lo * 2048 + xl;
    const int t4 = 4 * lo + (t & 3);
    const u32 x4 = L_X + ((t >> 3) * 5 + 4) * 2048 + (t4 >> 3) * 1024 + (t4 & 7) * 128 + ((((t >> 2) & 1) * 4 + G) << 4);
    f32x4 s0 = zero4, q0 = zero4, s4 = zero4, q4 = zero4;
    v4u xs[2][3];
    float sq4 = 0.f;
    auto diag = [&](f32x4 v) { const float l_ = (t & 1) ? v[1] : v[0], h_ = (t & 1) ? v[3] : v[2]; return (t & 2) ? h_ : l_; };
    auto step = [&](auto I) {
      constexpr int be = decltype(I)::value;
      vmwait<issued_before_barrier(be) - need_before_barrier(be)>();
      BARRIER();
      if constexpr (be >= 4 && be <= 10) {
#pragma unroll
        for (int k = 0; k < 3; ++k) {
          const int i = 3 * (be - 4) + k, P = i / 5, kk = i % 5;
          if (i < 20) xs[be & 1][k] = kk < 4 ? lds128((xo0 ^ ((kk & 1) * 64)) + (2 * P + (kk >> 1)) * 10240) : lds128(x4 + P * 20480);
        }
      }
      if constexpr (be == 10) { xres[0] = lds64(xra); xres[1] = lds64(xra ^ 32); }
      SB0();
      item(IC(SCH[be + 1][0])); item(IC(SCH[be + 1][1]));
      SB0();
      if constexpr (be >= 5 && be <= 11) {
#pragma unroll
        for (int k = 0; k < 3; ++k) {
          constexpr int i0 = 3 * (be - 5);
          if (i0 + k < 20) {
            const v4u xx = xs[(be - 1) & 1][k];
            if ((i0 + k) % 5 < 4) { s0 = mfma32(ones, xx, s0); q0 = mfma32(xx, xx, q0); }
            else { s4 = mfma32(ones, xx, s4); q4 = mfma32(xx, xx, q4); }
          }
        }
      }
      if constexpr (be == 12) {
        float r, bb;
        ln_coef(s0[0], diag(q0), r, bb);
        if (G == (t >> 2)) *(LAS f32x2*)(C_R + (16 * lo + t) * 8) = f32x2{r, bb};
        sq4 = xg_sum(G == (t >> 2) ? diag(q4) : 0.f);
      }
      if constexpr (be == 13) {
        float r2, b2, sm = s4[0], sq = sq4;
        sm += dppf<0x128>(sm); sq += dppf<0x128>(sq); sm += dppf<0x124>(sm); sq += dppf<0x124>(sq);
        ln_coef(sm, sq, r2, b2);
        if (lane < 4) *(LAS f32x2*)(C_R + (64 + t4) * 8) = f32x2{r2, b2};
      }
    };
    UNROLL_SEQ(step, 16);
    vmwait<6>();
    __builtin_amdgcn_s_setprio(1);
    BARRIER();
#pragma unroll
    for (int ks = 2; ks < 6; ++ks) w1load(ks, (ks + 4) % 6);
    woread();
    VMWAIT0();
    lflag_set(FL_W1L + lo, 1);
    SB0();
    asm volatile("" : "+v"(epv));
    tag = mix64(((u64)uni((u32)(epv >> 32)) << 32) | uni((u32)epv));
    lwait4(FL_W1L, 1);
#pragma unroll
    for (int r = 0; r < 6; ++r) w1read(r, (r + 4) % 6);
    lflag_set(FL_W1C + lo, 1);
    lwait4(FL_W1C, 1);
#pragma unroll
    for (int ks = 6; ks < 12; ++ks) w1load(ks, ks - 6);
    __builtin_amdgcn_s_setprio(0);
  }
  v4u hres;
  {
    if (hi == 0) poll8(gw, G_FLA + lo * 8 * FSP, tag, lane); else VMWAIT0();
    BARRIER();
    const u32 c0 = hi * 32768 + lo * 4096;
#pragma unroll
    for (int i = 0; i < 2; ++i) dma4_sc1(gw + G_O + c0 + i * 16384, lane * 16, c0 + i * 16384);
    if (hi == 0) { w2read(0, L_W2 + lo * 4096); asm volatile("; w2 0" : "+v"(wr[3])); }
    v4u bf[2][4];
    f32x4 acc[2][4];
    u32 oa = c0 + lane * 8, ob = oa + 512;
    asm volatile("" : "+v"(oa), "+v"(ob));
    vmwait<4>();
#pragma unroll
    for (int s = 0; s < 4; ++s) bf[0][s] = act_frag(oa, ob, s * 1024);
    SB0();
#pragma unroll
    for (int s = 0; s < 2; ++s)
#pragma unroll
      for (int c = 0; c < 4; ++c) acc[0][c] = mfma32(wo[s].f[c], bf[0][s], s == 0 ? zero4 : acc[0][c]);
    SB0();
    VMWAIT0();
#pragma unroll
    for (int s = 0; s < 4; ++s) bf[1][s] = act_frag(oa, ob, 16384 + s * 1024);
    SB0();
#pragma unroll
    for (int s = 2; s < 4; ++s)
#pragma unroll
      for (int c = 0; c < 4; ++c) acc[0][c] = mfma32(wo[s].f[c], bf[0][s], acc[0][c]);
    SB0();
#pragma unroll
    for (int s = 0; s < 4; ++s)
#pragma unroll
      for (int c = 0; c < 4; ++c) acc[1][c] = mfma32(wo[s].f[c], bf[1][s], s == 0 ? zero4 : acc[1][c]);
#pragma unroll
    for (int c = 0; c < 4; ++c) *(LAS f32x4*)(c0 + c * 1024 + lane * 16) = acc[0][c];
    SB0();
#pragma unroll
    for (int c = 0; c < 4; ++c) *(LAS f32x4*)(c0 + 16384 + c * 1024 + lane * 16) = acc[1][c];
    if (hi == 0) w2cell(1, L_W2 + lo * 4096);
    BARRIER();
    const u32 pb = lo * 16384 + hi * 2048 + lane * 16;
    f32x4 hv[2];
#pragma unroll
    for (int c = 0; c < 2; ++c)
      hv[c] = ((ldsf4(pb + c * 1024) + ldsf4(pb + 4096 + c * 1024)) + (ldsf4(pb + 8192 + c * 1024) + ldsf4(pb + 12288 + c * 1024))) + unpk4(xres[c].x, xres[c].y);
    const v2u h0 = pk4(hv[0]), h1 = pk4(hv[1]);
    hres = v4u{h0.x, h0.y, h1.x, h1.y};
    __builtin_amdgcn_raw_buffer_store_b128(hres, rws, lane * 16, G_H + (lo * 16 + 2 * j + hi) * 1024, 16);
    SB0();
    const f32x4 s4 = hv[0] + hv[1], q4 = hv[0] * hv[0] + hv[1] * hv[1];
    const float sm = xg_sum((s4[0] + s4[1]) + (s4[2] + s4[3])), sq = xg_sum((q4[0] + q4[1]) + (q4[2] + q4[3]));
    if (lane < 16) __builtin_amdgcn_raw_buffer_store_b64(v2u{asu(sm), asu(sq)}, rws, lane * 8, G_ST + (j >> 1) * 2048 + lo * 512 + ((j & 1) * 2 + hi) * 128, 16);
    SB0();
  }
  const u32 fbo = G_FLB + ((lo & 1) * 32 + j * 4 + (lo >> 1) * 2 + hi) * FSP;
  f32x4 cs[3] = {zero4, zero4, zero4};
  if (hi) {
    *(LAS v4u*)(C_HR + lo * 1024 + lane * 16) = hres;
#pragma unroll
    for (int r = 6; r < 12; ++r) w1read(r, r - 6);
  }
  gflag_pub(gw, fbo, tag, lane);
  if (hi == 0) { w2read(1, L_W2 + lo * 4096); asm volatile("; w2 1" : "+v"(wr[7])); }
  BARRIER();
  w1load(12 + 2 * hi, 2 * hi); w1load(13 + 2 * hi, 2 * hi + 1);
  if (hi == 0) { w2cell(2, L_W2 + lo * 4096); w2cell(3, L_RING + 4 * SLOT + lo * 4096); }
  if (hi) {
#pragma unroll
    for (int r = 0; r < 12; ++r)
#pragma unroll
      for (int u = 0; u < 3; ++u) cs[u] = mfma32(wr[3 * r + u], ones, cs[u]);
  }
  poll8(gw, G_FLB + (hi * 32 + lo * 8) * FSP, tag, lane);
  dma4_sc1(gw + G_H + (hi * 16 + 4 * lo) * 1024, lane * 16, hi * 16384 + lo * 4096);
  dma4_sc1(gw + G_H + ((hi + 2) * 16 + 4 * lo) * 1024, lane * 16, (hi + 2) * 16384 + lo * 4096);
  BARRIER();
  dma1_sc1(gw + G_ST + w * 1024, lane * 16, C_S2 + w * 1024);
  int tz = tid;
  auto pollf = [&](int i) {
    const u64 v = __hip_atomic_load((GAS u64*)(gw + G_FLC + lo * 8 * FSP + i * FBL + (u32)(tz & 7) * FSP), __ATOMIC_RELAXED, __HIP_MEMORY_SCOPE_AGENT);
    const bool r = (__builtin_amdgcn_ballot_w64(v == tag) & 0xff) == 0xff; MEMFENCE(); return r;
  };
  auto waitf = [&](int i) {
#pragma unroll 1
    for (u32 s = 0; s < SPIN_G && !pollf(i); ++s) {}
  };
  auto fslot = [&](int i) -> u32 { return i == 2 ? lo * FPART : L_W2 + lo * AWP + (i == 1 ? FPART : 0); };
  auto fload = [&](int i) {
    const char* src = gw + G_F + i * F_TAU + lo * FPART;
    const u32 dst = fslot(i);
    const u32 l16 = (tz & 63) * 16;
    dma8_sc1(src + 4096, l16, dst + 4096); dma2_sc1(src + 8192, l16, dst + 8192);
    if (lo < 3) dma_more2048_sc1(src + 8192, l16);
  };
  char* const op = p.out + (size_t)T0 * 1024 + j * 128;
  auto fin = [&](int i, u32 buf, u32 pitch, int tok) {
    const int lane = tz & 63;
    const u32 pr = buf + (tok + (lane >> 5)) * 272 + (lane & 31) * 8;
    const f32x2 sv = (ldsf2(pr) + ldsf2(pr + pitch)) + (ldsf2(pr + 2 * pitch) + ldsf2(pr + 3 * pitch));
    *(u32*)(op + (u32)((16 * i + tok + (lane >> 5)) * 1024 + (lane & 31) * 4)) = pk2(sv[0], sv[1]);
  };
  asm volatile("" : "+s"(hr_));

  if (hr_ == 0) {
    asm volatile("" : "+v"(tz));
    const int lane = tz & 63, t = lane & 15, G = lane >> 4;
    const u32 wev = wcell_voff(lane, j), ro = wcell_roff(lane);
    u32 rw = ro;
    asm volatile("" : "+v"(rw));
    auto w2rd = [&](int s, u32 cell) {
      const u32 ad = cell + rw;
#pragma unroll
      for (int k = 0; k < 4; ++k) wr[4 * s + k] = trfrag(ad ^ (k * 32), 512);
    };
    w2rd(2, L_W2 + lo * 4096); w2rd(3, L_RING + 4 * SLOT + lo * 4096);
    vmwait<5>();
    BARRIER();
    const u32 aw = L_W2 + lo * AWW;
    auto w2c = [&](int s) { const char* src = w2s + ((s == 10 && lo == 3) ? 9 : s) * 32768; wcell_dma_half(src, wev, aw + (s % 5) * 4096, 0); wcell_dma_half(src, wev, aw + (s % 5) * 4096, 1); };
    VMWAIT0();
    w2c(4); w2c(5);
    BARRIER();
    {
      const u32 sta = C_S2 + G * 2048 + lo * 512 + t * 8;
      const f32x2 a_ = (ldsf2(sta) + ldsf2(sta + 128)) + (ldsf2(sta + 256) + ldsf2(sta + 384));
      const float sm = xg_sum(a_[0]);
      float r, bb; ln_coef(sm, xg_sum(a_[1]), r, bb);
      if (lane < 16) *(LAS f32x2*)(C_TB + (lo * 16 + t) * 8) = f32x2{r, bb};
      lflag_set(FL_COEF + lo, 1);
    }
    w2c(6); w2c(7); w2c(8);
    vmwait<12>();
    w2rd(4, aw + 4 * 4096); w2rd(5, aw);
    LGWAIT0();
    w2c(9); w2c(10);
    vmwait<8>();
    w2rd(6, aw + 4096); w2rd(7, aw + 2 * 4096); w2rd(8, aw + 3 * 4096);
    VMWAIT0();
    w2rd(9, aw + 4 * 4096); w2rd(10, aw);
    const v4u hre2 = lds128(C_HR + lo * 1024 + lane * 16);
    lflag_set(FL_W2D + lo, 1);
    if (lane == 0) __hip_atomic_store((GAS u64*)(gw + G_FLA + (j * 4 + lo) * FSP), ~tag, __ATOMIC_RELAXED, __HIP_MEMORY_SCOPE_AGENT);
    MEMFENCE();
    lwait4(FL_W2D, 1);
    waitf(0);
    fload(0);
    __builtin_amdgcn_s_setprio(2);
    const u32 pw = t * 272 + G * 16;
    f32x4 acc[4][4];
    auto init = [&](int i) {
      const u32 mk = (i == lo) ? 0xffffffffu : 0u;
      acc[i][0] = unpk4(hres.x & mk, hres.y & mk); acc[i][1] = unpk4(hres.z & mk, hres.w & mk);
      acc[i][2] = unpk4(hre2.x & mk, hre2.y & mk); acc[i][3] = unpk4(hre2.z & mk, hre2.w & mk);
      asm volatile("" : "+v"(acc[i][0]), "+v"(acc[i][1]), "+v"(acc[i][2]), "+v"(acc[i][3]));
    };
    constexpr int PD = 4;
    v4u bf[4][11];
    u32 sa[3] = {fslot(0) + lane * 8, fslot(1) + lane * 8, fslot(2) + lane * 8};
    asm volatile("" : "+v"(sa[0]), "+v"(sa[1]), "+v"(sa[2]));
    auto rd = [&](int i, int k) { bf[i][k] = act_frag(sa[i % 3], sa[i % 3] + 512, k * 1024); SB0(); };
    auto dump = [&](int i) {
      const u32 buf = (i == 0 ? L_P4 + lo * PQ : fslot(i)) + pw;
#pragma unroll
      for (int c = 0; c < 4; ++c) *(LAS f32x4*)(buf + c * 64) = acc[i][c];
      lflag_set(FL_A0 + lo, i + 1);
    };
    auto tile = [&](auto I) {
      constexpr int i = decltype(I)::value;
#pragma unroll
      for (int k = 0; k < 10; ++k) {
        acc[i][0] = mfma32(wr[4 * k], bf[i][k], acc[i][0]);
        SB0();
        if (k + PD < 11) rd(i, k + PD);
        if (k == 1 && i > 1) dump(i - 1);
        if (k == 3 && i < 3) init(i + 1);
        SB0();
#pragma unroll
        for (int c = 1; c < 4; ++c) acc[i][c] = mfma32(wr[4 * k + c], bf[i][k], acc[i][c]);
        SB0();
      }
      if (lo < 3) {
#pragma unroll
        for (int c = 0; c < 4; ++c) acc[i][c] = mfma32(wr[40 + c], bf[i][10], acc[i][c]);
      }
      SB0();
      if constexpr (i < 3) {
        if constexpr (i == 0) dump(0);
        lwait1(FL_FRDY + lo, i + 1);
#pragma unroll
        for (int k = 0; k < PD; ++k) rd(i + 1, k);
      }
    };
    init(0);
    lwait4(FL_BDONE, 4);
    VMWAIT0();
#pragma unroll
    for (int k = 0; k < PD; ++k) rd(0, k);
    UNROLL_SEQ(tile, 4);
    dump(3);
    BARRIER();
    fin(3, L_W2, AWP, 2 * w);
    if (w == 0 && j == 0 && lane == 0) __hip_atomic_store((GAS u64*)(gw + G_EPOCH), tag, __ATOMIC_RELAXED, __HIP_MEMORY_SCOPE_AGENT);
  } else {
#pragma unroll
    for (int r = 12; r < 16; ++r) w1read(r, r - 12);
    SB0();
#pragma unroll
    for (int r = 12; r < 16; ++r)
#pragma unroll
      for (int u = 0; u < 3; ++u) cs[u] = mfma32(wr[3 * r + u], ones, cs[u]);
    if (t == 0) {
#pragma unroll
      for (int u = 0; u < 3; ++u) *(LAS f32x4*)(C_CS1 + (lo * 3 + u) * 64 + G * 16) = cs[u];
    }
    __builtin_amdgcn_s_setprio(3);
    vmwait<5>();
    BARRIER();
    const u32 fco = G_FLC + (j * 4 + lo) * FSP;
    int pend = 0;
    auto pubto = [&](int stored) {
      VMWAIT0();
      if (lane < stored - pend) __hip_atomic_store((GAS u64*)(gw + fco + (pend + lane) * FBL), tag, __ATOMIC_RELAXED, __HIP_MEMORY_SCOPE_AGENT);
      MEMFENCE();
    };
    {
      constexpr int PD = 3;
      v4u hf[64 + PD];
      f32x4 acc[4][3], c4;
      f32x2 rb;
      u32 csa = C_CS1 + lo * 192 + G * 16, tba = C_TB + t * 8;
      asm volatile("" : "+v"(csa), "+v"(tba));
      const u32 hl = lane * 16;
      auto epi = [&](int i, int u) {
        f32x4 v = acc[i][u] * rb[0] + c4 * rb[1];
        const v2u fo = {pk2(fmaxf(v[0], 0.f), fmaxf(v[1], 0.f)), pk2(fmaxf(v[2], 0.f), fmaxf(v[3], 0.f))};
        if (u < 2 || nv == 3) __builtin_amdgcn_raw_buffer_store_b64(fo, rws, lane * 8, G_F + i * F_TAU + (Tj + lt + u) * 512, 16);
      };
#pragma unroll
      for (int k = 0; k < PD; ++k) hf[k] = lds128(hl + k * 1024);
      auto p3step = [&](auto I) {
        constexpr int n = decltype(I)::value, i = n >> 4, ks = n & 15;
        if (ks == 0) mfma32z(acc[i][0], wr[3 * ks], hf[n]); else mfma32t(acc[i][0], wr[3 * ks], hf[n]);
        SB0();
        if constexpr (n == 9) { VMWAIT0(); BARRIER(); }
        if constexpr (n == 15) lwait4(FL_COEF, 1);
        if constexpr (n + PD < 64) hf[n + PD] = lds128(hl + (n + PD) * 1024);
        if constexpr (ks == 15) rb = ldsf2(tba + i * 128);
        SB0();
        if (ks == 0) mfma32z(acc[i][1], wr[3 * ks + 1], hf[n]); else mfma32t(acc[i][1], wr[3 * ks + 1], hf[n]);
        SB0();
        if constexpr (i > 1 && ks == 1) { pubto(i - 1); pend = i - 1; }
        if constexpr (i > 0 && ks >= 1 && ks <= 3) {
          if constexpr (ks == 1) asm volatile("s_nop 2" ::: "memory");
          epi(i - 1, ks - 1);
        }
        if constexpr (i > 0 && ks <= 2) c4 = ldsf4(csa + ks * 64);
        SB0();
        if (ks == 0) mfma32z(acc[i][2], wr[3 * ks + 2], hf[n]); else mfma32t(acc[i][2], wr[3 * ks + 2], hf[n]);
        SB0();
      };
      UNROLL_SEQ(p3step, 64);
      lflag_set(FL_BDONE + lo, 4);
      f32x4 cl[3];
#pragma unroll
      for (int u = 0; u < 3; ++u) cl[u] = ldsf4(csa + u * 64);
      pubto(3);
      pend = 3;
      asm volatile("s_nop 7" : "+v"(acc[3][0]), "+v"(acc[3][1]), "+v"(acc[3][2]));
#pragma unroll
      for (int u = 0; u < 3; ++u) { c4 = cl[u]; epi(3, u); }
    }
    asm volatile("" : "+v"(tz));
    bool g1 = pollf(1);
    pubto(4);
    __builtin_amdgcn_s_setprio(1);
    lwait4(FL_W2D, 1);
    if (!g1) waitf(1);
    fload(1);
    bool g2 = pollf(2);
    VMWAIT0(); lflag_set(FL_FRDY + lo, 1);
    if (!g2) waitf(2);
    lwait4(FL_BDONE, 4);
    fload(2);
    bool g3 = pollf(3);
    VMWAIT0(); lflag_set(FL_FRDY + lo, 2);
    if (!g3) waitf(3);
    lwait1(FL_A0 + lo, 1);
    fload(3);
    VMWAIT0(); lflag_set(FL_FRDY + lo, 3);
    lwait4(FL_A0, 3);
#pragma unroll
    for (int k = 0; k < 2; ++k) { fin(0, L_P4, PQ, 4 * lo + 2 * k); fin(1, L_W2 + FPART, AWP, 4 * lo + 2 * k); fin(2, 0, FPART, 4 * lo + 2 * k); }
    BARRIER();
    fin(3, L_W2, AWP, 2 * w);
  }
}
}

extern "C" void kernel_launch(void* const* d_in, const int* in_sizes, int n_in, void* d_out, int out_size, void* d_ws, size_t ws_size,
                              hipStream_t stream) {
  fb::Params p;
  p.x = (const char*)d_in[0]; p.wqkv = (const char*)d_in[1]; p.wo = (const char*)d_in[2];
  p.w1 = (const char*)d_in[4]; p.w2 = (const char*)d_in[5];
  p.out = (char*)d_out; p.ws = (char*)d_ws;
  (void)hipFuncSetAttribute((const void*)fb::fused_block, hipFuncAttributeMaxDynamicSharedMemorySize, fb::L_TOTAL);
  fb::fused_block<<<256, 512, fb::L_TOTAL, stream>>>(p);
}
```
